```python
import torch

def _softmax(x, axis=-1):
    return torch.softmax(x, dim=axis)
def _gelu(x):
    return torch.nn.functional.gelu(x, approximate="tanh")
def _silu(x):
    return torch.nn.functional.silu(x)
def _sigmoid(x):
    return torch.sigmoid(x)
def _layernorm(x, axis=-1, eps=1e-5):
    m = x.mean(dim=axis, keepdim=True)
    v = x.var(dim=axis, keepdim=True, unbiased=False)
    return (x - m) / torch.sqrt(v + eps)
def _scatter_add(tgt, idx, src, axis=0):
    return tgt.clone().index_add_(axis, idx, src)
def _segment_sum(src, seg, n):
    out = torch.zeros((n,) + src.shape[1:], dtype=src.dtype)
    return out.index_add_(0, seg, src)
def _pad(x, pw):
    flat = tuple(v for pair in reversed(pw) for v in pair)
    return torch.nn.functional.pad(x, flat)

kernel_name = 'standalone__self_sup_conv_stem_hubert__speech'

def setup_inputs():

    _w = (lambda a: (lambda t: t.to(torch.bfloat16) if t.is_floating_point() else t)(torch.as_tensor(a)))
    B, T, D, K = 2, 4000, 512, 15
    scale = (1.0 / (K * D)) ** 0.5
    return {
        "x": _w((lambda *s: torch.randn(*s, dtype=torch.bfloat16) * 0.05)(B, T, D)),
        "w": _w((lambda *s: torch.randn(*s, dtype=torch.bfloat16) * 0.05)(K, D, D) * scale),
        "b": _w((lambda *s: torch.randn(*s, dtype=torch.bfloat16) * 0.05)(D) * 0.1),
        "gamma": _w(torch.ones((D,), dtype=float)),
        "beta": _w(torch.zeros((D,), dtype=float)),
    }

def reference(x, w, b, gamma, beta):
    B, T, D = x.shape
    K = w.shape[0]
    s = 2
    L = (T - K) // s + 1
    y = torch.einsum('blc,cd->bld', x[:, 0 : s * L : s, :], w[0])
    for i in range(1, K):
        y = y + torch.einsum('blc,cd->bld', x[:, i : i + s * L : s, :], w[i])
    y = y + b[None, None, :]
    mu = y.mean(1, keepdims=True)
    var = ((y - mu) ** 2).mean(1, keepdims=True)
    yn = (y - mu) / torch.sqrt(var + 1e-5)
    yn = yn * gamma[None, None, :] + beta[None, None, :]
    return _gelu(yn)
```

```cpp
#include <hip/hip_runtime.h>
#include <hip/hip_bf16.h>
typedef short bf16x8 __attribute__((ext_vector_type(8)));
typedef float f32x4 __attribute__((ext_vector_type(4)));
constexpr int Bn=2, T=4000, D=512, KT=15, L=1993;

__device__ __forceinline__ float bf2f(unsigned short u){ return __uint_as_float(((unsigned)u)<<16); }

__global__ __launch_bounds__(256) void conv_k(const unsigned short* __restrict__ x, const unsigned short* __restrict__ w,
                                              const unsigned short* __restrict__ bias, float* __restrict__ y) {
  __shared__ __attribute__((aligned(16))) unsigned short As[128][40];
  __shared__ __attribute__((aligned(16))) unsigned short Bs[64][40];
  int bm = blockIdx.x, bn = blockIdx.y;
  int b = bm / 16, l0 = (bm % 16) * 128, n0 = bn * 64;
  int tid = threadIdx.x, wid = tid >> 6, lane = tid & 63;
  int fr = lane & 15, fq = lane >> 4;
  f32x4 acc[2][4] = {};
  const unsigned short* xb = x + (size_t)b * T * D;
  for (int kk = 0; kk < KT * D; kk += 32) {
    for (int i = tid; i < 512; i += 256) {
      int r = i >> 2, c = (i & 3) * 8;
      int l = l0 + r; if (l > L - 1) l = L - 1;
      bf16x8 v = *(const bf16x8*)(xb + (size_t)l * 2 * D + kk + c);
      *(bf16x8*)&As[r][c] = v;
    }
    {
      int k = tid >> 3, c = (tid & 7) * 8;
      bf16x8 v = *(const bf16x8*)(w + (size_t)(kk + k) * D + n0 + c);
      for (int j = 0; j < 8; ++j) Bs[c + j][k] = (unsigned short)v[j];
    }
    __syncthreads();
    bf16x8 a[2], bb[4];
    for (int m = 0; m < 2; ++m) a[m] = *(const bf16x8*)&As[wid * 32 + m * 16 + fr][fq * 8];
    for (int n = 0; n < 4; ++n) bb[n] = *(const bf16x8*)&Bs[n * 16 + fr][fq * 8];
    for (int m = 0; m < 2; ++m) for (int n = 0; n < 4; ++n)
      acc[m][n] = __builtin_amdgcn_mfma_f32_16x16x32_bf16(a[m], bb[n], acc[m][n], 0, 0, 0);
    __syncthreads();
  }
  for (int m = 0; m < 2; ++m) for (int n = 0; n < 4; ++n) for (int j = 0; j < 4; ++j) {
    int l = l0 + wid * 32 + m * 16 + fq * 4 + j, d = n0 + n * 16 + fr;
    if (l < L) y[((size_t)b * L + l) * D + d] = acc[m][n][j] + bf2f(bias[d]);
  }
}
__global__ __launch_bounds__(256) void stats_k(const float* __restrict__ y, float* __restrict__ st) {
  __shared__ float red[4][64];
  int b = blockIdx.x, d = blockIdx.y * 64 + (threadIdx.x & 63), g = threadIdx.x >> 6;
  const float* yb = y + (size_t)b * L * D + d;
  float s = 0; for (int l = g; l < L; l += 4) s += yb[(size_t)l * D];
  red[g][threadIdx.x & 63] = s; __syncthreads();
  float mu = (red[0][threadIdx.x & 63] + red[1][threadIdx.x & 63] + red[2][threadIdx.x & 63] + red[3][threadIdx.x & 63]) / L;
  __syncthreads();
  float q = 0; for (int l = g; l < L; l += 4) { float t = yb[(size_t)l * D] - mu; q += t * t; }
  red[g][threadIdx.x & 63] = q; __syncthreads();
  float var = (red[0][threadIdx.x & 63] + red[1][threadIdx.x & 63] + red[2][threadIdx.x & 63] + red[3][threadIdx.x & 63]) / L;
  if (g == 0) { st[(b * D + d) * 2] = mu; st[(b * D + d) * 2 + 1] = 1.0f / sqrtf(var + 1e-5f); }
}
__global__ __launch_bounds__(256) void norm_k(const float* __restrict__ y, const float* __restrict__ st, const unsigned short* __restrict__ gamma,
                                              const unsigned short* __restrict__ beta, __hip_bfloat16* __restrict__ out) {
  size_t i = (size_t)blockIdx.x * 256 + threadIdx.x;
  if (i >= (size_t)Bn * L * D) return;
  int d = i % D; int b = i / ((size_t)L * D);
  float v = (y[i] - st[(b * D + d) * 2]) * st[(b * D + d) * 2 + 1] * bf2f(gamma[d]) + bf2f(beta[d]);
  float u = 0.7978845608028654f * (v + 0.044715f * v * v * v);
  float g = 0.5f * v * (1.0f + tanhf(u));
  out[i] = __float2bfloat16(g);
}
extern "C" void kernel_launch(void* const* d_in, const int* in_sizes, int n_in, void* d_out, int out_size, void* d_ws, size_t ws_size, hipStream_t stream) {
  const unsigned short* x = (const unsigned short*)d_in[0];
  const unsigned short* w = (const unsigned short*)d_in[1];
  const unsigned short* bias = (const unsigned short*)d_in[2];
  const unsigned short* gamma = (const unsigned short*)d_in[3];
  const unsigned short* beta = (const unsigned short*)d_in[4];
  float* y = (float*)d_ws; float* st = y + (size_t)Bn * L * D;
  conv_k<<<dim3(32, 8), 256, 0, stream>>>(x, w, bias, y);
  stats_k<<<dim3(Bn, D / 64), 256, 0, stream>>>(y, st);
  norm_k<<<(Bn * L * D + 255) / 256, 256, 0, stream>>>(y, st, gamma, beta, (__hip_bfloat16*)d_out);
}
```

```cpp
#include <hip/hip_runtime.h>
constexpr int LDS_BYTES = 163840;

__global__ __launch_bounds__(256, 1) void conv_gn_gelu_k(const unsigned short* x, const unsigned short* w,
                                                         const unsigned short* bias, const unsigned short* gamma,
                                                         const unsigned short* beta, unsigned short* out,
                                                         char* ws) {
  asm volatile(
"s_load_dwordx2 s[12:13], %0, 0x8\n\t"
"s_load_dwordx2 s[8:9], %0, 0x0\n\t"
"s_load_dwordx4 s[28:31], %0, 0x10\n\t"
"s_load_dwordx2 s[4:5], %0, 0x20\n\t"
"s_load_dwordx2 s[20:21], %0, 0x28\n\t"
"s_load_dwordx2 s[16:17], %0, 0x30\n\t"
"v_lshrrev_b32 v3, 6, %2\n\t"
"v_and_b32 v1, 63, %2\n\t"
"v_readfirstlane_b32 s34, v3\n\t"
"s_lshr_b32 s36, %1, 3\n\t"
"s_lshr_b32 s41, %1, 6\n\t"
"v_and_b32 v2, 3, v1\n\t"
"v_lshlrev_b32 v22, 7, v2\n\t"
"v_bfe_u32 v2, v1, 2, 1\n\t"
"v_lshl_or_b32 v22, v2, 10, v22\n\t"
"v_bfe_u32 v2, v1, 3, 1\n\t"
"v_lshl_or_b32 v22, v2, 20, v22\n\t"
"s_bfe_u32 s40, %1, 0x10001\n\t"
"s_lshl_b32 s40, s40, 3\n\t"
"s_and_b32 s42, s36, 7\n\t"
"s_add_u32 s38, s40, s42\n\t"
"s_bfe_u32 s39, %1, 0x10002\n\t"
"s_mul_i32 s27, s39, 4096000\n\t"
"s_add_u32 s10, s27, 4096000\n\t"
"s_lshl_b32 s40, s38, 18\n\t"
"s_add_u32 s27, s27, s40\n\t"
"s_mov_b32 s14, 7864320\n\t"
"s_mov_b32 s15, 0x20000\n\t"
"s_mov_b32 s11, 0x20000\n\t"
"s_lshl2_add_u32 s40, s41, s34\n\t"
"s_mul_i32 s40, s40, 34\n\t"
"v_add_u32 v2, s40, v1\n\t"
"s_and_b32 s42, %1, 1\n\t"
"s_lshl2_add_u32 s37, s42, s41\n\t"
"s_lshl2_add_u32 s43, s36, s42\n\t"
"s_lshl_b32 s43, s43, 9\n\t"
"v_and_b32 v23, 3, v2\n\t"
"v_lshrrev_b32 v2, 2, v2\n\t"
"v_lshlrev_b32 v23, 8, v23\n\t"
"v_lshl_or_b32 v23, v2, 11, v23\n\t"
"s_lshl_b32 s25, s34, 17\n\t"
"s_add_u32 s43, s43, s25\n\t"
"s_lshl2_add_u32 s40, s37, s34\n\t"
"s_lshl_b32 s40, s40, 5\n\t"
"v_lshrrev_b32 v3, 4, v1\n\t"
"v_lshl_add_u32 v25, v3, 3, s40\n\t"
"s_mov_b64 exec, 0xffff\n\t"
"s_waitcnt lgkmcnt(0)\n\t"
"buffer_load_dword v24, v22, s[12:15], s43 offen\n\t"
"s_mov_b64 exec, -1\n\t"
"s_xor_b32 s43, s27, 0\n\t"
"s_mov_b32 exec_hi, 3\n\t"
"buffer_load_dword v24, v23, s[8:11], s43 offen\n\t"
"s_mov_b32 exec_hi, -1\n\t"
"global_load_dwordx2 v[36:37], v25, s[28:29]\n\t"
"s_lshl_b32 s40, s37, 7\n\t"
"s_add_u32 s25, s25, s40\n\t"
"s_mul_i32 s26, s34, 40960\n\t"
"s_lshl_b32 s40, s34, 8\n\t"
"s_add_u32 s24, s27, s40\n\t"
"s_xor_b32 s6, s25, 65536\n\t"
"s_xor_b32 s7, s24, 128\n\t"
"s_mov_b32 s33, 0\n\t"
"v_lshrrev_b32 v2, 5, v1\n\t"
"v_lshlrev_b32 v4, 13, v2\n\t"
"v_bfe_u32 v2, v1, 3, 2\n\t"
"v_lshl_or_b32 v4, v2, 10, v4\n\t"
"v_and_b32 v2, 7, v1\n\t"
"v_lshl_or_b32 v4, v2, 4, v4\n\t"
"v_lshrrev_b32 v2, 4, v1\n\t"
"v_lshlrev_b32 v2, 5, v2\n\t"
"v_xor_b32 v4, v2, v4\n\t"
"v_add_u32 v5, 3072, v4\n\t"
"v_add_u32 v6, 14336, v4\n\t"
"v_add_u32 v7, 17408, v4\n\t"
"v_and_b32 v2, 7, v1\n\t"
"v_lshlrev_b32 v3, 4, v2\n\t"
"v_lshrrev_b32 v2, 4, v1\n\t"
"v_lshlrev_b32 v2, 4, v2\n\t"
"v_xor_b32 v3, v2, v3\n\t"
"v_lshrrev_b32 v2, 3, v1\n\t"
"v_lshl_or_b32 v8, v2, 14, v3\n\t"
"v_lshl_or_b32 v10, v2, 11, v3\n\t"
"v_xor_b32 v9, 64, v8\n\t"
"v_add_u32 v9, 130048, v9\n\t"
"s_add_u32 m0, s26, 24576\n\t"
"s_add_u32 s40, s25, 0\n\t"
"buffer_load_dwordx4 v4, s[12:15], s40 offen lds\n\t"
"buffer_load_dwordx4 v5, s[12:15], s40 offen offset:1024 lds\n\t"
"buffer_load_dwordx4 v6, s[12:15], s40 offen offset:2048 lds\n\t"
"buffer_load_dwordx4 v7, s[12:15], s40 offen offset:3072 lds\n\t"
"v_lshrrev_b32 v3, 2, v1\n\t"
"s_lshl2_add_u32 s35, s36, s34\n\t"
"s_cmp_ge_u32 s36, 24\n\t"
"s_cselect_b32 s35, 0x1000, s35\n\t"
"v_mul_u32_u24 v2, 96, v3\n\t"
"v_add_u32 v2, s35, v2\n\t"
"v_cmp_eq_u32 vcc, 5, v3\n\t"
"s_and_b32 s43, s35, 31\n\t"
"s_add_u32 s43, s43, 480\n\t"
"v_mov_b32 v99, s43\n\t"
"v_cndmask_b32 v2, v2, v99, vcc\n\t"
"v_cndmask_b32 v98, 0, -1, vcc\n\t"
"v_and_b32 v22, 63, v2\n\t"
"v_lshlrev_b32 v22, 10, v22\n\t"
"v_bfe_u32 v99, v2, 6, 2\n\t"
"v_lshl_or_b32 v22, v99, 17, v22\n\t"
"v_bfe_u32 v99, v2, 8, 1\n\t"
"v_lshl_or_b32 v22, v99, 20, v22\n\t"
"v_lshrrev_b32 v99, 9, v2\n\t"
"v_lshl_or_b32 v22, v99, 24, v22\n\t"
"v_and_b32 v99, 3, v1\n\t"
"v_lshl_or_b32 v22, v99, 7, v22\n\t"
"s_and_b32 s43, %1, 1\n\t"
"s_lshl_b32 s43, s43, 9\n\t"
"v_or_b32 v22, s43, v22\n\t"
"s_lshr_b32 s35, %1, 6\n\t"
"s_cmp_eq_u32 s35, 1\n\t"
"s_cselect_b32 s43, 0, 0x1000000\n\t"
"v_and_or_b32 v96, v98, s43, v22\n\t"
"s_cmp_eq_u32 s35, 2\n\t"
"s_cselect_b32 s43, 0, 0x1000000\n\t"
"v_and_or_b32 v97, v98, s43, v22\n\t"
"s_cmp_eq_u32 s35, 0\n\t"
"s_cselect_b32 s43, 0, 0x1000000\n\t"
"v_and_or_b32 v22, v98, s43, v22\n\t"
"v_and_b32 v2, 3, v1\n\t"
"v_lshlrev_b32 v11, 7, v2\n\t"
"v_lshrrev_b32 v2, 2, v1\n\t"
"v_lshl_or_b32 v11, v2, 10, v11\n\t"
"s_lshl_b32 s43, s34, 14\n\t"
"s_and_b32 s3, s36, 3\n\t"
"s_lshl_b32 s3, s3, 17\n\t"
"s_or_b32 s43, s43, s3\n\t"
"s_bfe_u32 s3, s36, 0x10002\n\t"
"s_lshl_b32 s3, s3, 20\n\t"
"s_or_b32 s43, s43, s3\n\t"
"s_and_b32 s3, %1, 1\n\t"
"s_lshl_b32 s3, s3, 9\n\t"
"s_or_b32 s43, s43, s3\n\t"
"v_or_b32 v11, s43, v11\n\t"
"s_cmp_eq_u32 s35, 0\n\t"
"s_cselect_b32 s94, 0, 0x10000000\n\t"
"s_cmp_eq_u32 s35, 1\n\t"
"s_cselect_b32 s95, 0, 0x10000000\n\t"
"s_cmp_eq_u32 s35, 2\n\t"
"s_cselect_b32 s96, 0, 0x10000000\n\t"
"s_cmp_eq_u32 s35, 3\n\t"
"s_cselect_b32 s97, 0, 0x10000000\n\t"
"s_add_u32 m0, s26, 0\n\t"
"s_add_u32 s41, s24, 0\n\t"
"buffer_load_dwordx4 v8, s[8:11], s41 offen lds\n\t"
"buffer_load_dwordx4 v9, s[8:11], s41 offen offset:1024 lds\n\t"
"buffer_load_dwordx4 v8, s[8:11], s41 offen offset:2048 lds\n\t"
"buffer_load_dwordx4 v9, s[8:11], s41 offen offset:3072 lds\n\t"
"s_add_u32 m0, s26, 4096\n\t"
"s_add_u32 s42, s24, 8192\n\t"
"buffer_load_dwordx4 v8, s[8:11], s42 offen lds\n\t"
"buffer_load_dwordx4 v9, s[8:11], s42 offen offset:1024 lds\n\t"
"buffer_load_dwordx4 v8, s[8:11], s42 offen offset:2048 lds\n\t"
"buffer_load_dwordx4 v9, s[8:11], s42 offen offset:3072 lds\n\t"
"s_add_u32 m0, s26, 20480\n\t"
"s_add_u32 s40, s25, 32768\n\t"
"buffer_load_dwordx4 v4, s[12:15], s40 offen lds\n\t"
"buffer_load_dwordx4 v5, s[12:15], s40 offen offset:1024 lds\n\t"
"buffer_load_dwordx4 v6, s[12:15], s40 offen offset:2048 lds\n\t"
"buffer_load_dwordx4 v7, s[12:15], s40 offen offset:3072 lds\n\t"
"s_add_u32 m0, s26, 8192\n\t"
"s_add_u32 s41, s24, 4096\n\t"
"buffer_load_dwordx4 v8, s[8:11], s41 offen lds\n\t"
"buffer_load_dwordx4 v9, s[8:11], s41 offen offset:1024 lds\n\t"
"buffer_load_dwordx4 v8, s[8:11], s41 offen offset:2048 lds\n\t"
"buffer_load_dwordx4 v9, s[8:11], s41 offen offset:3072 lds\n\t"
"s_add_u32 m0, s26, 16384\n\t"
"s_add_u32 s42, s25, 1048576\n\t"
"buffer_load_dwordx4 v4, s[12:15], s42 offen lds\n\t"
"buffer_load_dwordx4 v5, s[12:15], s42 offen offset:1024 lds\n\t"
"buffer_load_dwordx4 v6, s[12:15], s42 offen offset:2048 lds\n\t"
"buffer_load_dwordx4 v7, s[12:15], s42 offen offset:3072 lds\n\t"
"v_lshrrev_b32 v2, 5, v1\n\t"
"v_lshlrev_b32 v12, 11, v2\n\t"
"v_bfe_u32 v2, v1, 4, 1\n\t"
"v_lshl_or_b32 v12, v2, 9, v12\n\t"
"v_bfe_u32 v2, v1, 2, 2\n\t"
"v_lshl_or_b32 v12, v2, 7, v12\n\t"
"v_bfe_u32 v2, v1, 4, 1\n\t"
"v_lshl_or_b32 v12, v2, 6, v12\n\t"
"v_bfe_u32 v2, v1, 3, 1\n\t"
"v_lshl_or_b32 v12, v2, 5, v12\n\t"
"v_and_b32 v2, 3, v1\n\t"
"v_lshl_or_b32 v12, v2, 3, v12\n\t"
"s_xor_b32 s43, s34, 3\n\t"
"s_lshl_b32 s43, s43, 5\n\t"
"s_add_u32 s43, s43, s26\n\t"
"v_xor_b32 v12, s43, v12\n\t"
"v_xor_b32 v13, 32, v12\n\t"
"v_xor_b32 v14, 64, v12\n\t"
"v_xor_b32 v15, 96, v12\n\t"
"v_and_b32 v2, 15, v1\n\t"
"v_lshlrev_b32 v16, 7, v2\n\t"
"v_lshrrev_b32 v2, 4, v1\n\t"
"v_lshl_or_b32 v16, v2, 4, v16\n\t"
"v_bfe_u32 v2, v1, 1, 3\n\t"
"v_lshlrev_b32 v2, 4, v2\n\t"
"v_xor_b32 v16, v2, v16\n\t"
"v_add_u32 v16, s26, v16\n\t"
"v_xor_b32 v17, 64, v16\n\t"
"s_waitcnt vmcnt(20)\n\t"
"ds_read_b64_tr_b16 a[96:97], v12 offset:24576\n\t"
"ds_read_b64_tr_b16 a[98:99], v12 offset:25600\n\t"
"ds_read_b64_tr_b16 a[100:101], v13 offset:24576\n\t"
"ds_read_b64_tr_b16 a[102:103], v13 offset:25600\n\t"
"ds_read_b64_tr_b16 a[104:105], v14 offset:24576\n\t"
"ds_read_b64_tr_b16 a[106:107], v14 offset:25600\n\t"
"ds_read_b64_tr_b16 a[108:109], v15 offset:24576\n\t"
"ds_read_b64_tr_b16 a[110:111], v15 offset:25600\n\t"
"s_waitcnt lgkmcnt(0)\n\t"
"s_waitcnt vmcnt(16)\n\t"
"ds_read_b128 v[116:119], v16 offset:0\n\t"
"ds_read_b128 v[120:123], v17 offset:0\n\t"
"ds_read_b128 v[124:127], v16 offset:2048\n\t"
"ds_read_b128 v[128:131], v17 offset:2048\n\t"
"v_lshlrev_b32 v32, 16, v36\n\t"
"v_and_b32 v33, 0xffff0000, v36\n\t"
"v_lshlrev_b32 v34, 16, v37\n\t"
"v_and_b32 v35, 0xffff0000, v37\n\t"
"s_waitcnt lgkmcnt(0)\n\t"
"v_mfma_f32_16x16x32_bf16 a[0:3], a[96:99], v[116:119], 0\n\t"
"v_mfma_f32_16x16x32_bf16 a[4:7], a[100:103], v[116:119], 0\n\t"
"s_waitcnt vmcnt(12)\n\t"
"ds_read_b128 v[132:135], v16 offset:4096\n\t"
"ds_read_b128 v[136:139], v17 offset:4096\n\t"
"ds_read_b128 v[140:143], v16 offset:6144\n\t"
"ds_read_b128 v[144:147], v17 offset:6144\n\t"
"v_mfma_f32_16x16x32_bf16 a[8:11], a[104:107], v[116:119], 0\n\t"
"s_add_u32 m0, s26, 32768\n\t"
"s_add_u32 s40, s25, 1081344\n\t"
"buffer_load_dwordx4 v4, s[12:15], s40 offen lds\n\t"
"v_mfma_f32_16x16x32_bf16 v[64:67], a[108:111], v[116:119], v[32:35]\n\t"
"v_mfma_f32_16x16x32_bf16 a[12:15], a[96:99], v[124:127], 0\n\t"
"v_mfma_f32_16x16x32_bf16 a[16:19], a[100:103], v[124:127], 0\n\t"
"v_mfma_f32_16x16x32_bf16 a[20:23], a[104:107], v[124:127], 0\n\t"
"buffer_load_dwordx4 v5, s[12:15], s40 offen offset:1024 lds\n\t"
"v_mfma_f32_16x16x32_bf16 v[68:71], a[108:111], v[124:127], v[32:35]\n\t"
"s_waitcnt lgkmcnt(0)\n\t"
"v_mfma_f32_16x16x32_bf16 a[48:51], a[96:99], v[132:135], 0\n\t"
"v_mfma_f32_16x16x32_bf16 a[52:55], a[100:103], v[132:135], 0\n\t"
"s_waitcnt vmcnt(10)\n\t"
"ds_read_b64_tr_b16 a[112:113], v12 offset:20480\n\t"
"ds_read_b64_tr_b16 a[114:115], v12 offset:21504\n\t"
"ds_read_b64_tr_b16 a[116:117], v13 offset:20480\n\t"
"ds_read_b64_tr_b16 a[118:119], v13 offset:21504\n\t"
"ds_read_b64_tr_b16 a[120:121], v14 offset:20480\n\t"
"ds_read_b64_tr_b16 a[122:123], v14 offset:21504\n\t"
"ds_read_b64_tr_b16 a[124:125], v15 offset:20480\n\t"
"ds_read_b64_tr_b16 a[126:127], v15 offset:21504\n\t"
"v_mfma_f32_16x16x32_bf16 a[56:59], a[104:107], v[132:135], 0\n\t"
"buffer_load_dwordx4 v6, s[12:15], s40 offen offset:2048 lds\n\t"
"v_mfma_f32_16x16x32_bf16 v[80:83], a[108:111], v[132:135], v[32:35]\n\t"
"v_mfma_f32_16x16x32_bf16 a[60:63], a[96:99], v[140:143], 0\n\t"
"v_mfma_f32_16x16x32_bf16 a[64:67], a[100:103], v[140:143], 0\n\t"
"v_mfma_f32_16x16x32_bf16 a[68:71], a[104:107], v[140:143], 0\n\t"
"buffer_load_dwordx4 v7, s[12:15], s40 offen offset:3072 lds\n\t"
"v_mfma_f32_16x16x32_bf16 v[84:87], a[108:111], v[140:143], v[32:35]\n\t"
"s_waitcnt lgkmcnt(0)\n\t"
"v_mfma_f32_16x16x32_bf16 a[0:3], a[112:115], v[120:123], a[0:3]\n\t"
"v_mfma_f32_16x16x32_bf16 a[4:7], a[116:119], v[120:123], a[4:7]\n\t"
"v_mfma_f32_16x16x32_bf16 a[8:11], a[120:123], v[120:123], a[8:11]\n\t"
"s_xor_b32 s43, s33, 2097152\n\t"
"buffer_load_dword v24, v96, s[12:15], s43 offen\n\t"
"v_mfma_f32_16x16x32_bf16 v[64:67], a[124:127], v[120:123], v[64:67]\n\t"
"v_mfma_f32_16x16x32_bf16 a[12:15], a[112:115], v[128:131], a[12:15]\n\t"
"v_mfma_f32_16x16x32_bf16 a[16:19], a[116:119], v[128:131], a[16:19]\n\t"
"v_mfma_f32_16x16x32_bf16 a[20:23], a[120:123], v[128:131], a[20:23]\n\t"
"s_add_u32 m0, s26, 36864\n\t"
"s_add_u32 s41, s24, 12288\n\t"
"buffer_load_dwordx4 v8, s[8:11], s41 offen lds\n\t"
"v_mfma_f32_16x16x32_bf16 v[68:71], a[124:127], v[128:131], v[68:71]\n\t"
"v_mfma_f32_16x16x32_bf16 a[48:51], a[112:115], v[136:139], a[48:51]\n\t"
"v_mfma_f32_16x16x32_bf16 a[52:55], a[116:119], v[136:139], a[52:55]\n\t"
"s_waitcnt vmcnt(10)\n\t"
"ds_read_b128 v[148:151], v16 offset:8192\n\t"
"ds_read_b128 v[152:155], v17 offset:8192\n\t"
"ds_read_b128 v[156:159], v16 offset:10240\n\t"
"ds_read_b128 v[160:163], v17 offset:10240\n\t"
"v_mfma_f32_16x16x32_bf16 a[56:59], a[120:123], v[136:139], a[56:59]\n\t"
"buffer_load_dwordx4 v9, s[8:11], s41 offen offset:1024 lds\n\t"
"v_mfma_f32_16x16x32_bf16 v[80:83], a[124:127], v[136:139], v[80:83]\n\t"
"v_mfma_f32_16x16x32_bf16 a[60:63], a[112:115], v[144:147], a[60:63]\n\t"
"v_mfma_f32_16x16x32_bf16 a[64:67], a[116:119], v[144:147], a[64:67]\n\t"
"v_mfma_f32_16x16x32_bf16 a[68:71], a[120:123], v[144:147], a[68:71]\n\t"
"buffer_load_dwordx4 v8, s[8:11], s41 offen offset:2048 lds\n\t"
"v_mfma_f32_16x16x32_bf16 v[84:87], a[124:127], v[144:147], v[84:87]\n\t"
"s_waitcnt lgkmcnt(0)\n\t"
"v_mfma_f32_16x16x32_bf16 a[24:27], a[96:99], v[148:151], 0\n\t"
"v_mfma_f32_16x16x32_bf16 a[24:27], a[112:115], v[152:155], a[24:27]\n\t"
"v_mfma_f32_16x16x32_bf16 a[28:31], a[100:103], v[148:151], 0\n\t"
"buffer_load_dwordx4 v9, s[8:11], s41 offen offset:3072 lds\n\t"
"v_mfma_f32_16x16x32_bf16 a[28:31], a[116:119], v[152:155], a[28:31]\n\t"
"v_mfma_f32_16x16x32_bf16 a[32:35], a[104:107], v[148:151], 0\n\t"
"v_mfma_f32_16x16x32_bf16 a[32:35], a[120:123], v[152:155], a[32:35]\n\t"
"v_mfma_f32_16x16x32_bf16 v[72:75], a[108:111], v[148:151], v[32:35]\n\t"
"s_add_u32 m0, s26, 12288\n\t"
"s_add_u32 s42, s25, 2097152\n\t"
"buffer_load_dwordx4 v4, s[12:15], s42 offen lds\n\t"
"v_mfma_f32_16x16x32_bf16 v[72:75], a[124:127], v[152:155], v[72:75]\n\t"
"v_mfma_f32_16x16x32_bf16 a[36:39], a[96:99], v[156:159], 0\n\t"
"v_mfma_f32_16x16x32_bf16 a[36:39], a[112:115], v[160:163], a[36:39]\n\t"
"s_waitcnt vmcnt(10)\n\t"
"ds_read_b64_tr_b16 a[128:129], v12 offset:16384\n\t"
"ds_read_b64_tr_b16 a[130:131], v12 offset:17408\n\t"
"ds_read_b64_tr_b16 a[132:133], v13 offset:16384\n\t"
"ds_read_b64_tr_b16 a[134:135], v13 offset:17408\n\t"
"ds_read_b64_tr_b16 a[136:137], v14 offset:16384\n\t"
"ds_read_b64_tr_b16 a[138:139], v14 offset:17408\n\t"
"ds_read_b64_tr_b16 a[140:141], v15 offset:16384\n\t"
"ds_read_b64_tr_b16 a[142:143], v15 offset:17408\n\t"
"v_mfma_f32_16x16x32_bf16 a[40:43], a[100:103], v[156:159], 0\n\t"
"v_mfma_f32_16x16x32_bf16 a[40:43], a[116:119], v[160:163], a[40:43]\n\t"
"buffer_load_dwordx4 v5, s[12:15], s42 offen offset:1024 lds\n\t"
"v_mfma_f32_16x16x32_bf16 a[44:47], a[104:107], v[156:159], 0\n\t"
"v_mfma_f32_16x16x32_bf16 a[44:47], a[120:123], v[160:163], a[44:47]\n\t"
"v_mfma_f32_16x16x32_bf16 v[76:79], a[108:111], v[156:159], v[32:35]\n\t"
"v_mfma_f32_16x16x32_bf16 v[76:79], a[124:127], v[160:163], v[76:79]\n\t"
"s_waitcnt lgkmcnt(0)\n\t"
"v_mfma_f32_16x16x32_bf16 a[0:3], a[128:131], v[124:127], a[0:3]\n\t"
"buffer_load_dwordx4 v6, s[12:15], s42 offen offset:2048 lds\n\t"
"v_mfma_f32_16x16x32_bf16 a[4:7], a[132:135], v[124:127], a[4:7]\n\t"
"v_mfma_f32_16x16x32_bf16 a[8:11], a[136:139], v[124:127], a[8:11]\n\t"
"v_mfma_f32_16x16x32_bf16 v[64:67], a[140:143], v[124:127], v[64:67]\n\t"
"v_mfma_f32_16x16x32_bf16 a[12:15], a[128:131], v[148:151], a[12:15]\n\t"
"v_mfma_f32_16x16x32_bf16 a[16:19], a[132:135], v[148:151], a[16:19]\n\t"
"buffer_load_dwordx4 v7, s[12:15], s42 offen offset:3072 lds\n\t"
"v_mfma_f32_16x16x32_bf16 a[20:23], a[136:139], v[148:151], a[20:23]\n\t"
"v_mfma_f32_16x16x32_bf16 v[68:71], a[140:143], v[148:151], v[68:71]\n\t"
"v_mfma_f32_16x16x32_bf16 a[24:27], a[128:131], v[156:159], a[24:27]\n\t"
"v_mfma_f32_16x16x32_bf16 a[28:31], a[132:135], v[156:159], a[28:31]\n\t"
"v_mfma_f32_16x16x32_bf16 a[32:35], a[136:139], v[156:159], a[32:35]\n\t"
"s_add_u32 m0, s26, 28672\n\t"
"s_add_u32 s40, s24, 262144\n\t"
"buffer_load_dwordx4 v10, s[8:11], s40 offen lds\n\t"
"v_mfma_f32_16x16x32_bf16 v[72:75], a[140:143], v[156:159], v[72:75]\n\t"
"v_mfma_f32_16x16x32_bf16 a[36:39], a[128:131], v[132:135], a[36:39]\n\t"
"v_mfma_f32_16x16x32_bf16 a[40:43], a[132:135], v[132:135], a[40:43]\n\t"
"s_waitcnt vmcnt(10)\n\t"
"ds_read_b64_tr_b16 a[144:145], v12 offset:32768\n\t"
"ds_read_b64_tr_b16 a[146:147], v12 offset:33792\n\t"
"ds_read_b64_tr_b16 a[148:149], v13 offset:32768\n\t"
"ds_read_b64_tr_b16 a[150:151], v13 offset:33792\n\t"
"ds_read_b64_tr_b16 a[152:153], v14 offset:32768\n\t"
"ds_read_b64_tr_b16 a[154:155], v14 offset:33792\n\t"
"ds_read_b64_tr_b16 a[156:157], v15 offset:32768\n\t"
"ds_read_b64_tr_b16 a[158:159], v15 offset:33792\n\t"
"v_mfma_f32_16x16x32_bf16 a[44:47], a[136:139], v[132:135], a[44:47]\n\t"
"v_mfma_f32_16x16x32_bf16 v[76:79], a[140:143], v[132:135], v[76:79]\n\t"
"s_add_u32 m0, s26, 24576\n\t"
"s_add_u32 s41, s25, 2129920\n\t"
"buffer_load_dwordx4 v4, s[12:15], s41 offen lds\n\t"
"v_mfma_f32_16x16x32_bf16 a[48:51], a[128:131], v[140:143], a[48:51]\n\t"
"v_mfma_f32_16x16x32_bf16 a[52:55], a[132:135], v[140:143], a[52:55]\n\t"
"v_mfma_f32_16x16x32_bf16 a[56:59], a[136:139], v[140:143], a[56:59]\n\t"
"v_mfma_f32_16x16x32_bf16 v[80:83], a[140:143], v[140:143], v[80:83]\n\t"
"s_waitcnt lgkmcnt(0)\n\t"
"v_mfma_f32_16x16x32_bf16 a[0:3], a[144:147], v[128:131], a[0:3]\n\t"
"buffer_load_dwordx4 v5, s[12:15], s41 offen offset:1024 lds\n\t"
"v_mfma_f32_16x16x32_bf16 a[4:7], a[148:151], v[128:131], a[4:7]\n\t"
"v_mfma_f32_16x16x32_bf16 a[8:11], a[152:155], v[128:131], a[8:11]\n\t"
"v_mfma_f32_16x16x32_bf16 v[64:67], a[156:159], v[128:131], v[64:67]\n\t"
"v_mfma_f32_16x16x32_bf16 a[12:15], a[144:147], v[152:155], a[12:15]\n\t"
"v_mfma_f32_16x16x32_bf16 a[16:19], a[148:151], v[152:155], a[16:19]\n\t"
"buffer_load_dwordx4 v6, s[12:15], s41 offen offset:2048 lds\n\t"
"v_mfma_f32_16x16x32_bf16 a[20:23], a[152:155], v[152:155], a[20:23]\n\t"
"v_mfma_f32_16x16x32_bf16 v[68:71], a[156:159], v[152:155], v[68:71]\n\t"
"v_mfma_f32_16x16x32_bf16 a[24:27], a[144:147], v[160:163], a[24:27]\n\t"
"v_mfma_f32_16x16x32_bf16 a[28:31], a[148:151], v[160:163], a[28:31]\n\t"
"v_mfma_f32_16x16x32_bf16 a[32:35], a[152:155], v[160:163], a[32:35]\n\t"
"buffer_load_dwordx4 v7, s[12:15], s41 offen offset:3072 lds\n\t"
"v_mfma_f32_16x16x32_bf16 v[72:75], a[156:159], v[160:163], v[72:75]\n\t"
"v_mfma_f32_16x16x32_bf16 a[36:39], a[144:147], v[136:139], a[36:39]\n\t"
"v_mfma_f32_16x16x32_bf16 a[40:43], a[148:151], v[136:139], a[40:43]\n\t"
"s_waitcnt vmcnt(9)\n\t"
"ds_read_b128 v[164:167], v16 offset:36864\n\t"
"ds_read_b128 v[168:171], v17 offset:36864\n\t"
"ds_read_b128 v[172:175], v16 offset:38912\n\t"
"ds_read_b128 v[176:179], v17 offset:38912\n\t"
"v_mfma_f32_16x16x32_bf16 a[44:47], a[152:155], v[136:139], a[44:47]\n\t"
"v_mfma_f32_16x16x32_bf16 v[76:79], a[156:159], v[136:139], v[76:79]\n\t"
"s_add_u32 m0, s26, 0\n\t"
"s_add_u32 s42, s25, 3145728\n\t"
"buffer_load_dwordx4 v4, s[12:15], s42 offen lds\n\t"
"v_mfma_f32_16x16x32_bf16 a[48:51], a[144:147], v[144:147], a[48:51]\n\t"
"v_mfma_f32_16x16x32_bf16 a[52:55], a[148:151], v[144:147], a[52:55]\n\t"
"v_mfma_f32_16x16x32_bf16 a[56:59], a[152:155], v[144:147], a[56:59]\n\t"
"v_mfma_f32_16x16x32_bf16 v[80:83], a[156:159], v[144:147], v[80:83]\n\t"
"s_waitcnt lgkmcnt(0)\n\t"
"v_mfma_f32_16x16x32_bf16 a[60:63], a[128:131], v[164:167], a[60:63]\n\t"
"buffer_load_dwordx4 v5, s[12:15], s42 offen offset:1024 lds\n\t"
"v_mfma_f32_16x16x32_bf16 a[60:63], a[144:147], v[168:171], a[60:63]\n\t"
"v_mfma_f32_16x16x32_bf16 a[64:67], a[132:135], v[164:167], a[64:67]\n\t"
"v_mfma_f32_16x16x32_bf16 a[64:67], a[148:151], v[168:171], a[64:67]\n\t"
"v_mfma_f32_16x16x32_bf16 a[68:71], a[136:139], v[164:167], a[68:71]\n\t"
"v_mfma_f32_16x16x32_bf16 a[68:71], a[152:155], v[168:171], a[68:71]\n\t"
"buffer_load_dwordx4 v6, s[12:15], s42 offen offset:2048 lds\n\t"
"v_mfma_f32_16x16x32_bf16 v[84:87], a[140:143], v[164:167], v[84:87]\n\t"
"v_mfma_f32_16x16x32_bf16 v[84:87], a[156:159], v[168:171], v[84:87]\n\t"
"v_mfma_f32_16x16x32_bf16 a[72:75], a[96:99], v[164:167], 0\n\t"
"v_mfma_f32_16x16x32_bf16 a[72:75], a[112:115], v[168:171], a[72:75]\n\t"
"v_mfma_f32_16x16x32_bf16 a[72:75], a[128:131], v[172:175], a[72:75]\n\t"
"buffer_load_dwordx4 v7, s[12:15], s42 offen offset:3072 lds\n\t"
"v_mfma_f32_16x16x32_bf16 a[72:75], a[144:147], v[176:179], a[72:75]\n\t"
"v_mfma_f32_16x16x32_bf16 a[76:79], a[100:103], v[164:167], 0\n\t"
"v_mfma_f32_16x16x32_bf16 a[76:79], a[116:119], v[168:171], a[76:79]\n\t"
"v_mfma_f32_16x16x32_bf16 a[76:79], a[132:135], v[172:175], a[76:79]\n\t"
"v_mfma_f32_16x16x32_bf16 a[76:79], a[148:151], v[176:179], a[76:79]\n\t"
"s_waitcnt vmcnt(8)\n\t"
"ds_read_b128 v[100:103], v16 offset:28672\n\t"
"ds_read_b128 v[104:107], v17 offset:28672\n\t"
"s_xor_b32 s43, s94, 4194304\n\t"
"buffer_load_dword v24, v11, s[12:15], s43 offen\n\t"
"ds_read_b64_tr_b16 a[160:161], v12 offset:12288\n\t"
"v_mfma_f32_16x16x32_bf16 a[80:83], a[104:107], v[164:167], 0\n\t"
"v_mfma_f32_16x16x32_bf16 a[80:83], a[120:123], v[168:171], a[80:83]\n\t"
"ds_read_b64_tr_b16 a[162:163], v12 offset:13312\n\t"
"v_mfma_f32_16x16x32_bf16 a[80:83], a[136:139], v[172:175], a[80:83]\n\t"
"v_mfma_f32_16x16x32_bf16 a[80:83], a[152:155], v[176:179], a[80:83]\n\t"
"ds_read_b64_tr_b16 a[164:165], v13 offset:12288\n\t"
"v_mfma_f32_16x16x32_bf16 v[88:91], a[108:111], v[164:167], v[32:35]\n\t"
"s_waitcnt lgkmcnt(3)\n\t"
"v_mov_b32_dpp v180, v100 row_shr:15 row_mask:0xf bank_mask:0x8\n\t"
"v_mov_b32_dpp v181, v101 row_shr:15 row_mask:0xf bank_mask:0x8\n\t"
"s_xor_b32 s43, s95, 6291456\n\t"
"buffer_load_dword v24, v11, s[12:15], s43 offen\n\t"
"v_mfma_f32_16x16x32_bf16 v[88:91], a[124:127], v[168:171], v[88:91]\n\t"
"v_mov_b32_dpp v182, v102 row_shr:15 row_mask:0xf bank_mask:0x8\n\t"
"v_mov_b32_dpp v183, v103 row_shr:15 row_mask:0xf bank_mask:0x8\n\t"
"ds_read_b64_tr_b16 a[166:167], v13 offset:13312\n\t"
"v_mfma_f32_16x16x32_bf16 v[88:91], a[140:143], v[172:175], v[88:91]\n\t"
"v_mov_b32_dpp v180, v116 row_shl:1 row_mask:0xf bank_mask:0xf\n\t"
"v_mov_b32_dpp v181, v117 row_shl:1 row_mask:0xf bank_mask:0xf\n\t"
"v_mfma_f32_16x16x32_bf16 v[88:91], a[156:159], v[176:179], v[88:91]\n\t"
"v_mov_b32_dpp v182, v118 row_shl:1 row_mask:0xf bank_mask:0xf\n\t"
"v_mov_b32_dpp v183, v119 row_shl:1 row_mask:0xf bank_mask:0xf\n\t"
"ds_read_b64_tr_b16 a[168:169], v14 offset:12288\n\t"
"v_mfma_f32_16x16x32_bf16 a[84:87], a[96:99], v[172:175], 0\n\t"
"v_mov_b32_dpp v184, v104 row_shr:15 row_mask:0xf bank_mask:0x8\n\t"
"v_mov_b32_dpp v185, v105 row_shr:15 row_mask:0xf bank_mask:0x8\n\t"
"v_mfma_f32_16x16x32_bf16 a[84:87], a[112:115], v[176:179], a[84:87]\n\t"
"v_mov_b32_dpp v186, v106 row_shr:15 row_mask:0xf bank_mask:0x8\n\t"
"v_mov_b32_dpp v187, v107 row_shr:15 row_mask:0xf bank_mask:0x8\n\t"
"s_add_u32 m0, s26, 4096\n\t"
"s_add_u32 s40, s25, 3178496\n\t"
"buffer_load_dwordx4 v4, s[12:15], s40 offen lds\n\t"
"ds_read_b64_tr_b16 a[170:171], v14 offset:13312\n\t"
"v_mfma_f32_16x16x32_bf16 a[88:91], a[100:103], v[172:175], 0\n\t"
"v_mov_b32_dpp v184, v120 row_shl:1 row_mask:0xf bank_mask:0xf\n\t"
"v_mov_b32_dpp v185, v121 row_shl:1 row_mask:0xf bank_mask:0xf\n\t"
"v_mfma_f32_16x16x32_bf16 a[88:91], a[116:119], v[176:179], a[88:91]\n\t"
"v_mov_b32_dpp v186, v122 row_shl:1 row_mask:0xf bank_mask:0xf\n\t"
"v_mov_b32_dpp v187, v123 row_shl:1 row_mask:0xf bank_mask:0xf\n\t"
"ds_read_b64_tr_b16 a[172:173], v15 offset:12288\n\t"
"v_mfma_f32_16x16x32_bf16 a[92:95], a[104:107], v[172:175], 0\n\t"
"v_mfma_f32_16x16x32_bf16 a[92:95], a[120:123], v[176:179], a[92:95]\n\t"
"ds_read_b64_tr_b16 a[174:175], v15 offset:13312\n\t"
"v_mfma_f32_16x16x32_bf16 v[92:95], a[108:111], v[172:175], v[32:35]\n\t"
"buffer_load_dwordx4 v5, s[12:15], s40 offen offset:1024 lds\n\t"
"v_mfma_f32_16x16x32_bf16 v[92:95], a[124:127], v[176:179], v[92:95]\n\t"
"s_nop 1\n\t"
"v_mfma_f32_16x16x32_bf16 a[84:87], a[128:131], v[180:183], a[84:87]\n\t"
"v_mfma_f32_16x16x32_bf16 a[84:87], a[144:147], v[184:187], a[84:87]\n\t"
"v_mfma_f32_16x16x32_bf16 a[88:91], a[132:135], v[180:183], a[88:91]\n\t"
"v_mfma_f32_16x16x32_bf16 a[88:91], a[148:151], v[184:187], a[88:91]\n\t"
"buffer_load_dwordx4 v6, s[12:15], s40 offen offset:2048 lds\n\t"
"v_mfma_f32_16x16x32_bf16 a[92:95], a[136:139], v[180:183], a[92:95]\n\t"
"v_mfma_f32_16x16x32_bf16 a[92:95], a[152:155], v[184:187], a[92:95]\n\t"
"v_mfma_f32_16x16x32_bf16 v[92:95], a[140:143], v[180:183], v[92:95]\n\t"
"v_mfma_f32_16x16x32_bf16 v[92:95], a[156:159], v[184:187], v[92:95]\n\t"
"buffer_load_dwordx4 v7, s[12:15], s40 offen offset:3072 lds\n\t"
"s_waitcnt lgkmcnt(0)\n\t"
"v_mov_b32_dpp v188, v100 row_shr:14 row_mask:0xf bank_mask:0x8\n\t"
"v_mfma_f32_16x16x32_bf16 a[0:3], a[160:163], v[148:151], a[0:3]\n\t"
"s_add_u32 m0, s26, 20480\n\t"
"s_add_u32 s41, s25, 4194304\n\t"
"v_mfma_f32_16x16x32_bf16 a[4:7], a[164:167], v[148:151], a[4:7]\n\t"
"buffer_load_dwordx4 v4, s[12:15], s41 offen lds\n\t"
"v_mfma_f32_16x16x32_bf16 a[8:11], a[168:171], v[148:151], a[8:11]\n\t"
"v_mov_b32_dpp v189, v101 row_shr:14 row_mask:0xf bank_mask:0x8\n\t"
"v_mfma_f32_16x16x32_bf16 v[64:67], a[172:175], v[148:151], v[64:67]\n\t"
"v_mov_b32_dpp v190, v102 row_shr:14 row_mask:0xf bank_mask:0x8\n\t"
"v_mfma_f32_16x16x32_bf16 v[68:71], a[172:175], v[156:159], v[68:71]\n\t"
"v_mov_b32_dpp v191, v103 row_shr:14 row_mask:0xf bank_mask:0x8\n\t"
"v_mfma_f32_16x16x32_bf16 a[20:23], a[168:171], v[156:159], a[20:23]\n\t"
"buffer_load_dwordx4 v5, s[12:15], s41 offen offset:1024 lds\n\t"
"v_mfma_f32_16x16x32_bf16 a[16:19], a[164:167], v[156:159], a[16:19]\n\t"
"v_mov_b32_dpp v188, v124 row_shl:1 row_mask:0xf bank_mask:0xf\n\t"
"v_mfma_f32_16x16x32_bf16 a[12:15], a[160:163], v[156:159], a[12:15]\n\t"
"v_mov_b32_dpp v189, v125 row_shl:1 row_mask:0xf bank_mask:0xf\n\t"
"v_mfma_f32_16x16x32_bf16 a[24:27], a[160:163], v[132:135], a[24:27]\n\t"
"v_mov_b32_dpp v190, v126 row_shl:1 row_mask:0xf bank_mask:0xf\n\t"
"v_mfma_f32_16x16x32_bf16 a[28:31], a[164:167], v[132:135], a[28:31]\n\t"
"buffer_load_dwordx4 v6, s[12:15], s41 offen offset:2048 lds\n\t"
"v_mfma_f32_16x16x32_bf16 a[32:35], a[168:171], v[132:135], a[32:35]\n\t"
"s_waitcnt vmcnt(13)\n\t"
"ds_read_b64_tr_b16 a[176:177], v12 offset:24576\n\t"
"v_mfma_f32_16x16x32_bf16 v[72:75], a[172:175], v[132:135], v[72:75]\n\t"
"v_mov_b32_dpp v191, v127 row_shl:1 row_mask:0xf bank_mask:0xf\n\t"
"v_mfma_f32_16x16x32_bf16 v[76:79], a[172:175], v[140:143], v[76:79]\n\t"
"ds_read_b64_tr_b16 a[178:179], v12 offset:25600\n\t"
"v_mfma_f32_16x16x32_bf16 a[44:47], a[168:171], v[140:143], a[44:47]\n\t"
"buffer_load_dwordx4 v7, s[12:15], s41 offen offset:3072 lds\n\t"
"v_mov_b32_dpp v192, v100 row_shr:13 row_mask:0xf bank_mask:0x8\n\t"
"v_mfma_f32_16x16x32_bf16 a[40:43], a[164:167], v[140:143], a[40:43]\n\t"
"ds_read_b64_tr_b16 a[180:181], v13 offset:24576\n\t"
"v_mfma_f32_16x16x32_bf16 a[36:39], a[160:163], v[140:143], a[36:39]\n\t"
"v_mov_b32_dpp v193, v101 row_shr:13 row_mask:0xf bank_mask:0x8\n\t"
"v_mfma_f32_16x16x32_bf16 a[48:51], a[160:163], v[164:167], a[48:51]\n\t"
"s_add_u32 m0, s26, 8192\n\t"
"s_add_u32 s42, s25, 4227072\n\t"
"ds_read_b64_tr_b16 a[182:183], v13 offset:25600\n\t"
"v_mfma_f32_16x16x32_bf16 a[52:55], a[164:167], v[164:167], a[52:55]\n\t"
"buffer_load_dwordx4 v4, s[12:15], s42 offen lds\n\t"
"v_mfma_f32_16x16x32_bf16 a[56:59], a[168:171], v[164:167], a[56:59]\n\t"
"ds_read_b64_tr_b16 a[184:185], v14 offset:24576\n\t"
"v_mfma_f32_16x16x32_bf16 v[80:83], a[172:175], v[164:167], v[80:83]\n\t"
"v_mov_b32_dpp v194, v102 row_shr:13 row_mask:0xf bank_mask:0x8\n\t"
"v_mfma_f32_16x16x32_bf16 v[84:87], a[172:175], v[172:175], v[84:87]\n\t"
"ds_read_b64_tr_b16 a[186:187], v14 offset:25600\n\t"
"v_mfma_f32_16x16x32_bf16 a[68:71], a[168:171], v[172:175], a[68:71]\n\t"
"buffer_load_dwordx4 v5, s[12:15], s42 offen offset:1024 lds\n\t"
"v_mov_b32_dpp v195, v103 row_shr:13 row_mask:0xf bank_mask:0x8\n\t"
"v_mfma_f32_16x16x32_bf16 a[64:67], a[164:167], v[172:175], a[64:67]\n\t"
"ds_read_b64_tr_b16 a[188:189], v15 offset:24576\n\t"
"v_mfma_f32_16x16x32_bf16 a[60:63], a[160:163], v[172:175], a[60:63]\n\t"
"v_mov_b32_dpp v192, v148 row_shl:1 row_mask:0xf bank_mask:0xf\n\t"
"v_mfma_f32_16x16x32_bf16 a[72:75], a[160:163], v[180:183], a[72:75]\n\t"
"ds_read_b64_tr_b16 a[190:191], v15 offset:25600\n\t"
"v_mfma_f32_16x16x32_bf16 a[76:79], a[164:167], v[180:183], a[76:79]\n\t"
"buffer_load_dwordx4 v6, s[12:15], s42 offen offset:2048 lds\n\t"
"v_mfma_f32_16x16x32_bf16 a[80:83], a[168:171], v[180:183], a[80:83]\n\t"
"v_mov_b32_dpp v193, v149 row_shl:1 row_mask:0xf bank_mask:0xf\n\t"
"v_mfma_f32_16x16x32_bf16 v[88:91], a[172:175], v[180:183], v[88:91]\n\t"
"v_mov_b32_dpp v194, v150 row_shl:1 row_mask:0xf bank_mask:0xf\n\t"
"v_mfma_f32_16x16x32_bf16 v[92:95], a[172:175], v[188:191], v[92:95]\n\t"
"v_mov_b32_dpp v195, v151 row_shl:1 row_mask:0xf bank_mask:0xf\n\t"
"v_mfma_f32_16x16x32_bf16 a[92:95], a[168:171], v[188:191], a[92:95]\n\t"
"buffer_load_dwordx4 v7, s[12:15], s42 offen offset:3072 lds\n\t"
"v_mfma_f32_16x16x32_bf16 a[88:91], a[164:167], v[188:191], a[88:91]\n\t"
"v_mfma_f32_16x16x32_bf16 a[84:87], a[160:163], v[188:191], a[84:87]\n\t"
"s_waitcnt lgkmcnt(0)\n\t"
"v_mov_b32_dpp v196, v104 row_shr:14 row_mask:0xf bank_mask:0x8\n\t"
"v_mfma_f32_16x16x32_bf16 a[0:3], a[176:179], v[152:155], a[0:3]\n\t"
"s_add_u32 m0, s26, 16384\n\t"
"s_add_u32 s40, s25, 5242880\n\t"
"v_mfma_f32_16x16x32_bf16 a[4:7], a[180:183], v[152:155], a[4:7]\n\t"
"buffer_load_dwordx4 v4, s[12:15], s40 offen lds\n\t"
"v_mfma_f32_16x16x32_bf16 a[8:11], a[184:187], v[152:155], a[8:11]\n\t"
"s_waitcnt vmcnt(15)\n\t"
"ds_read_b64_tr_b16 a[192:193], v12 offset:0\n\t"
"v_mov_b32_dpp v197, v105 row_shr:14 row_mask:0xf bank_mask:0x8\n\t"
"v_mfma_f32_16x16x32_bf16 v[64:67], a[188:191], v[152:155], v[64:67]\n\t"
"ds_read_b64_tr_b16 a[194:195], v12 offset:1024\n\t"
"v_mov_b32_dpp v198, v106 row_shr:14 row_mask:0xf bank_mask:0x8\n\t"
"v_mfma_f32_16x16x32_bf16 v[68:71], a[188:191], v[160:163], v[68:71]\n\t"
"ds_read_b64_tr_b16 a[196:197], v13 offset:0\n\t"
"v_mov_b32_dpp v199, v107 row_shr:14 row_mask:0xf bank_mask:0x8\n\t"
"v_mfma_f32_16x16x32_bf16 a[20:23], a[184:187], v[160:163], a[20:23]\n\t"
"v_mfma_f32_16x16x32_bf16 a[16:19], a[180:183], v[160:163], a[16:19]\n\t"
"ds_read_b64_tr_b16 a[198:199], v13 offset:1024\n\t"
"v_mov_b32_dpp v196, v128 row_shl:1 row_mask:0xf bank_mask:0xf\n\t"
"v_mfma_f32_16x16x32_bf16 a[12:15], a[176:179], v[160:163], a[12:15]\n\t"
"ds_read_b64_tr_b16 a[200:201], v14 offset:0\n\t"
"v_mov_b32_dpp v197, v129 row_shl:1 row_mask:0xf bank_mask:0xf\n\t"
"v_mfma_f32_16x16x32_bf16 a[24:27], a[176:179], v[136:139], a[24:27]\n\t"
"ds_read_b64_tr_b16 a[202:203], v14 offset:1024\n\t"
"v_mov_b32_dpp v198, v130 row_shl:1 row_mask:0xf bank_mask:0xf\n\t"
"v_mfma_f32_16x16x32_bf16 a[28:31], a[180:183], v[136:139], a[28:31]\n\t"
"buffer_load_dwordx4 v5, s[12:15], s40 offen offset:1024 lds\n\t"
"v_mfma_f32_16x16x32_bf16 a[32:35], a[184:187], v[136:139], a[32:35]\n\t"
"ds_read_b64_tr_b16 a[204:205], v15 offset:0\n\t"
"v_mfma_f32_16x16x32_bf16 v[72:75], a[188:191], v[136:139], v[72:75]\n\t"
"ds_read_b64_tr_b16 a[206:207], v15 offset:1024\n\t"
"v_mov_b32_dpp v199, v131 row_shl:1 row_mask:0xf bank_mask:0xf\n\t"
"v_mfma_f32_16x16x32_bf16 v[76:79], a[188:191], v[144:147], v[76:79]\n\t"
"s_waitcnt vmcnt(10)\n\t"
"ds_read_b64_tr_b16 a[208:209], v12 offset:4096\n\t"
"v_mfma_f32_16x16x32_bf16 a[44:47], a[184:187], v[144:147], a[44:47]\n\t"
"v_mov_b32_dpp v200, v104 row_shr:13 row_mask:0xf bank_mask:0x8\n\t"
"v_mfma_f32_16x16x32_bf16 a[40:43], a[180:183], v[144:147], a[40:43]\n\t"
"ds_read_b64_tr_b16 a[210:211], v12 offset:5120\n\t"
"v_mfma_f32_16x16x32_bf16 a[36:39], a[176:179], v[144:147], a[36:39]\n\t"
"v_mov_b32_dpp v201, v105 row_shr:13 row_mask:0xf bank_mask:0x8\n\t"
"v_mfma_f32_16x16x32_bf16 a[48:51], a[176:179], v[168:171], a[48:51]\n\t"
"ds_read_b64_tr_b16 a[212:213], v13 offset:4096\n\t"
"v_mfma_f32_16x16x32_bf16 a[52:55], a[180:183], v[168:171], a[52:55]\n\t"
"buffer_load_dwordx4 v6, s[12:15], s40 offen offset:2048 lds\n\t"
"v_mfma_f32_16x16x32_bf16 a[56:59], a[184:187], v[168:171], a[56:59]\n\t"
"ds_read_b64_tr_b16 a[214:215], v13 offset:5120\n\t"
"v_mfma_f32_16x16x32_bf16 v[80:83], a[188:191], v[168:171], v[80:83]\n\t"
"v_mov_b32_dpp v202, v106 row_shr:13 row_mask:0xf bank_mask:0x8\n\t"
"v_mfma_f32_16x16x32_bf16 v[84:87], a[188:191], v[176:179], v[84:87]\n\t"
"ds_read_b64_tr_b16 a[216:217], v14 offset:4096\n\t"
"v_mfma_f32_16x16x32_bf16 a[68:71], a[184:187], v[176:179], a[68:71]\n\t"
"v_mov_b32_dpp v203, v107 row_shr:13 row_mask:0xf bank_mask:0x8\n\t"
"v_mfma_f32_16x16x32_bf16 a[64:67], a[180:183], v[176:179], a[64:67]\n\t"
"ds_read_b64_tr_b16 a[218:219], v14 offset:5120\n\t"
"v_mfma_f32_16x16x32_bf16 a[60:63], a[176:179], v[176:179], a[60:63]\n\t"
"ds_read_b64_tr_b16 a[220:221], v15 offset:4096\n\t"
"v_mov_b32_dpp v200, v152 row_shl:1 row_mask:0xf bank_mask:0xf\n\t"
"v_mfma_f32_16x16x32_bf16 a[72:75], a[176:179], v[184:187], a[72:75]\n\t"
"ds_read_b64_tr_b16 a[222:223], v15 offset:5120\n\t"
"v_mfma_f32_16x16x32_bf16 a[76:79], a[180:183], v[184:187], a[76:79]\n\t"
"buffer_load_dwordx4 v7, s[12:15], s40 offen offset:3072 lds\n\t"
"v_mfma_f32_16x16x32_bf16 a[80:83], a[184:187], v[184:187], a[80:83]\n\t"
"v_mov_b32_dpp v201, v153 row_shl:1 row_mask:0xf bank_mask:0xf\n\t"
"v_mfma_f32_16x16x32_bf16 v[88:91], a[188:191], v[184:187], v[88:91]\n\t"
"v_mov_b32_dpp v202, v154 row_shl:1 row_mask:0xf bank_mask:0xf\n\t"
"v_mfma_f32_16x16x32_bf16 v[92:95], a[188:191], v[196:199], v[92:95]\n\t"
"v_mov_b32_dpp v203, v155 row_shl:1 row_mask:0xf bank_mask:0xf\n\t"
"v_mfma_f32_16x16x32_bf16 a[92:95], a[184:187], v[196:199], a[92:95]\n\t"
"v_mfma_f32_16x16x32_bf16 a[88:91], a[180:183], v[196:199], a[88:91]\n\t"
"v_mfma_f32_16x16x32_bf16 a[84:87], a[176:179], v[196:199], a[84:87]\n\t"
"s_xor_b32 s43, s27, 128\n\t"
"s_mov_b32 exec_hi, 3\n\t"
"buffer_load_dword v24, v23, s[8:11], s43 offen\n\t"
"s_mov_b32 exec_hi, -1\n\t"
"s_xor_b32 s43, s97, 65536\n\t"
"buffer_load_dword v24, v11, s[12:15], s43 offen\n\t"
"s_waitcnt lgkmcnt(0)\n\t"
"v_mfma_f32_16x16x32_bf16 a[0:3], a[192:195], v[156:159], a[0:3]\n\t"
"s_add_u32 m0, s26, 32768\n\t"
"s_add_u32 s41, s25, 5275648\n\t"
"v_mfma_f32_16x16x32_bf16 a[0:3], a[208:211], v[160:163], a[0:3]\n\t"
"buffer_load_dwordx4 v4, s[12:15], s41 offen lds\n\t"
"v_mfma_f32_16x16x32_bf16 a[4:7], a[212:215], v[160:163], a[4:7]\n\t"
"v_mfma_f32_16x16x32_bf16 a[4:7], a[196:199], v[156:159], a[4:7]\n\t"
"v_mfma_f32_16x16x32_bf16 a[8:11], a[200:203], v[156:159], a[8:11]\n\t"
"v_mfma_f32_16x16x32_bf16 a[8:11], a[216:219], v[160:163], a[8:11]\n\t"
"v_mfma_f32_16x16x32_bf16 v[64:67], a[220:223], v[160:163], v[64:67]\n\t"
"v_mfma_f32_16x16x32_bf16 v[64:67], a[204:207], v[156:159], v[64:67]\n\t"
"v_mfma_f32_16x16x32_bf16 v[68:71], a[204:207], v[132:135], v[68:71]\n\t"
"v_mfma_f32_16x16x32_bf16 v[68:71], a[220:223], v[136:139], v[68:71]\n\t"
"buffer_load_dwordx4 v5, s[12:15], s41 offen offset:1024 lds\n\t"
"v_mfma_f32_16x16x32_bf16 a[20:23], a[216:219], v[136:139], a[20:23]\n\t"
"v_mfma_f32_16x16x32_bf16 a[20:23], a[200:203], v[132:135], a[20:23]\n\t"
"v_mov_b32_dpp v204, v100 row_shr:12 row_mask:0xf bank_mask:0x8\n\t"
"v_mfma_f32_16x16x32_bf16 a[16:19], a[196:199], v[132:135], a[16:19]\n\t"
"v_mfma_f32_16x16x32_bf16 a[16:19], a[212:215], v[136:139], a[16:19]\n\t"
"v_mov_b32_dpp v205, v101 row_shr:12 row_mask:0xf bank_mask:0x8\n\t"
"v_mfma_f32_16x16x32_bf16 a[12:15], a[208:211], v[136:139], a[12:15]\n\t"
"v_mfma_f32_16x16x32_bf16 a[12:15], a[192:195], v[132:135], a[12:15]\n\t"
"v_mov_b32_dpp v206, v102 row_shr:12 row_mask:0xf bank_mask:0x8\n\t"
"v_mfma_f32_16x16x32_bf16 a[24:27], a[192:195], v[140:143], a[24:27]\n\t"
"v_mfma_f32_16x16x32_bf16 a[24:27], a[208:211], v[144:147], a[24:27]\n\t"
"buffer_load_dwordx4 v6, s[12:15], s41 offen offset:2048 lds\n\t"
"v_mfma_f32_16x16x32_bf16 a[28:31], a[212:215], v[144:147], a[28:31]\n\t"
"v_mfma_f32_16x16x32_bf16 a[28:31], a[196:199], v[140:143], a[28:31]\n\t"
"v_mov_b32_dpp v207, v103 row_shr:12 row_mask:0xf bank_mask:0x8\n\t"
"v_mfma_f32_16x16x32_bf16 a[32:35], a[200:203], v[140:143], a[32:35]\n\t"
"v_mfma_f32_16x16x32_bf16 a[32:35], a[216:219], v[144:147], a[32:35]\n\t"
"v_mfma_f32_16x16x32_bf16 v[72:75], a[220:223], v[144:147], v[72:75]\n\t"
"v_mfma_f32_16x16x32_bf16 v[72:75], a[204:207], v[140:143], v[72:75]\n\t"
"v_mov_b32_dpp v204, v156 row_shl:1 row_mask:0xf bank_mask:0xf\n\t"
"v_mfma_f32_16x16x32_bf16 v[76:79], a[204:207], v[164:167], v[76:79]\n\t"
"v_mfma_f32_16x16x32_bf16 v[76:79], a[220:223], v[168:171], v[76:79]\n\t"
"buffer_load_dwordx4 v7, s[12:15], s41 offen offset:3072 lds\n\t"
"v_mfma_f32_16x16x32_bf16 a[44:47], a[216:219], v[168:171], a[44:47]\n\t"
"v_mov_b32_dpp v205, v157 row_shl:1 row_mask:0xf bank_mask:0xf\n\t"
"v_mfma_f32_16x16x32_bf16 a[44:47], a[200:203], v[164:167], a[44:47]\n\t"
"v_mov_b32_dpp v206, v158 row_shl:1 row_mask:0xf bank_mask:0xf\n\t"
"v_mfma_f32_16x16x32_bf16 a[40:43], a[196:199], v[164:167], a[40:43]\n\t"
"v_mov_b32_dpp v207, v159 row_shl:1 row_mask:0xf bank_mask:0xf\n\t"
"v_mfma_f32_16x16x32_bf16 a[40:43], a[212:215], v[168:171], a[40:43]\n\t"
"v_mfma_f32_16x16x32_bf16 a[36:39], a[208:211], v[168:171], a[36:39]\n\t"
"v_mfma_f32_16x16x32_bf16 a[36:39], a[192:195], v[164:167], a[36:39]\n\t"
"v_mfma_f32_16x16x32_bf16 a[48:51], a[192:195], v[172:175], a[48:51]\n\t"
"v_mfma_f32_16x16x32_bf16 a[48:51], a[208:211], v[176:179], a[48:51]\n\t"
"v_mfma_f32_16x16x32_bf16 a[52:55], a[212:215], v[176:179], a[52:55]\n\t"
"s_waitcnt vmcnt(14)\n\t"
"ds_read_b64_tr_b16 a[224:225], v12 offset:20480\n\t"
"v_mfma_f32_16x16x32_bf16 a[52:55], a[196:199], v[172:175], a[52:55]\n\t"
"ds_read_b64_tr_b16 a[226:227], v12 offset:21504\n\t"
"v_mfma_f32_16x16x32_bf16 a[56:59], a[200:203], v[172:175], a[56:59]\n\t"
"ds_read_b64_tr_b16 a[228:229], v13 offset:20480\n\t"
"v_mfma_f32_16x16x32_bf16 a[56:59], a[216:219], v[176:179], a[56:59]\n\t"
"v_mfma_f32_16x16x32_bf16 v[80:83], a[220:223], v[176:179], v[80:83]\n\t"
"ds_read_b64_tr_b16 a[230:231], v13 offset:21504\n\t"
"v_mfma_f32_16x16x32_bf16 v[80:83], a[204:207], v[172:175], v[80:83]\n\t"
"ds_read_b64_tr_b16 a[232:233], v14 offset:20480\n\t"
"v_mfma_f32_16x16x32_bf16 v[84:87], a[204:207], v[180:183], v[84:87]\n\t"
"ds_read_b64_tr_b16 a[234:235], v14 offset:21504\n\t"
"v_mfma_f32_16x16x32_bf16 v[84:87], a[220:223], v[184:187], v[84:87]\n\t"
"v_mfma_f32_16x16x32_bf16 a[68:71], a[216:219], v[184:187], a[68:71]\n\t"
"ds_read_b64_tr_b16 a[236:237], v15 offset:20480\n\t"
"v_mfma_f32_16x16x32_bf16 a[68:71], a[200:203], v[180:183], a[68:71]\n\t"
"ds_read_b64_tr_b16 a[238:239], v15 offset:21504\n\t"
"v_mov_b32_dpp v208, v104 row_shr:12 row_mask:0xf bank_mask:0x8\n\t"
"v_mfma_f32_16x16x32_bf16 a[64:67], a[196:199], v[180:183], a[64:67]\n\t"
"s_waitcnt vmcnt(10)\n\t"
"ds_read_b64_tr_b16 a[240:241], v12 offset:8192\n\t"
"v_mfma_f32_16x16x32_bf16 a[64:67], a[212:215], v[184:187], a[64:67]\n\t"
"v_mov_b32_dpp v209, v105 row_shr:12 row_mask:0xf bank_mask:0x8\n\t"
"v_mfma_f32_16x16x32_bf16 a[60:63], a[208:211], v[184:187], a[60:63]\n\t"
"ds_read_b64_tr_b16 a[242:243], v12 offset:9216\n\t"
"v_mfma_f32_16x16x32_bf16 a[60:63], a[192:195], v[180:183], a[60:63]\n\t"
"v_mov_b32_dpp v210, v106 row_shr:12 row_mask:0xf bank_mask:0x8\n\t"
"v_mfma_f32_16x16x32_bf16 a[72:75], a[192:195], v[188:191], a[72:75]\n\t"
"ds_read_b64_tr_b16 a[244:245], v13 offset:8192\n\t"
"v_mfma_f32_16x16x32_bf16 a[72:75], a[208:211], v[196:199], a[72:75]\n\t"
"v_mfma_f32_16x16x32_bf16 a[76:79], a[212:215], v[196:199], a[76:79]\n\t"
"ds_read_b64_tr_b16 a[246:247], v13 offset:9216\n\t"
"v_mfma_f32_16x16x32_bf16 a[76:79], a[196:199], v[188:191], a[76:79]\n\t"
"v_mov_b32_dpp v211, v107 row_shr:12 row_mask:0xf bank_mask:0x8\n\t"
"v_mfma_f32_16x16x32_bf16 a[80:83], a[200:203], v[188:191], a[80:83]\n\t"
"ds_read_b64_tr_b16 a[248:249], v14 offset:8192\n\t"
"v_mfma_f32_16x16x32_bf16 a[80:83], a[216:219], v[196:199], a[80:83]\n\t"
"v_mfma_f32_16x16x32_bf16 v[88:91], a[220:223], v[196:199], v[88:91]\n\t"
"ds_read_b64_tr_b16 a[250:251], v14 offset:9216\n\t"
"v_mfma_f32_16x16x32_bf16 v[88:91], a[204:207], v[188:191], v[88:91]\n\t"
"ds_read_b64_tr_b16 a[252:253], v15 offset:8192\n\t"
"v_mov_b32_dpp v208, v160 row_shl:1 row_mask:0xf bank_mask:0xf\n\t"
"v_mfma_f32_16x16x32_bf16 v[92:95], a[204:207], v[192:195], v[92:95]\n\t"
"ds_read_b64_tr_b16 a[254:255], v15 offset:9216\n\t"
"v_mfma_f32_16x16x32_bf16 v[92:95], a[220:223], v[200:203], v[92:95]\n\t"
"v_mfma_f32_16x16x32_bf16 a[92:95], a[216:219], v[200:203], a[92:95]\n\t"
"v_mov_b32_dpp v209, v161 row_shl:1 row_mask:0xf bank_mask:0xf\n\t"
"v_mfma_f32_16x16x32_bf16 a[92:95], a[200:203], v[192:195], a[92:95]\n\t"
"v_mov_b32_dpp v210, v162 row_shl:1 row_mask:0xf bank_mask:0xf\n\t"
"v_mfma_f32_16x16x32_bf16 a[88:91], a[196:199], v[192:195], a[88:91]\n\t"
"v_mov_b32_dpp v211, v163 row_shl:1 row_mask:0xf bank_mask:0xf\n\t"
"v_mfma_f32_16x16x32_bf16 a[88:91], a[212:215], v[200:203], a[88:91]\n\t"
"v_mfma_f32_16x16x32_bf16 a[84:87], a[208:211], v[200:203], a[84:87]\n\t"
"v_mfma_f32_16x16x32_bf16 a[84:87], a[192:195], v[192:195], a[84:87]\n\t"
"s_waitcnt lgkmcnt(0)\n\t"
"v_lshlrev_b32 v2, 4, v1\n\t"
"v_mfma_f32_16x16x32_bf16 a[0:3], a[224:227], v[132:135], a[0:3]\n\t"
"s_add_u32 m0, s26, 36864\n\t"
"s_add_u32 s42, s25, 6291456\n\t"
"v_mfma_f32_16x16x32_bf16 a[0:3], a[240:243], v[136:139], a[0:3]\n\t"
"buffer_load_dwordx4 v4, s[12:15], s42 offen lds\n\t"
"v_mfma_f32_16x16x32_bf16 a[4:7], a[244:247], v[136:139], a[4:7]\n\t"
"v_add_u32 v18, s26, v2\n\t"
"v_mfma_f32_16x16x32_bf16 a[4:7], a[228:231], v[132:135], a[4:7]\n\t"
"s_xor_b32 s35, s34, 3\n\t"
"s_mul_i32 s35, s35, 40960\n\t"
"v_mfma_f32_16x16x32_bf16 a[8:11], a[232:235], v[132:135], a[8:11]\n\t"
"v_add_u32 v19, s35, v2\n\t"
"v_mfma_f32_16x16x32_bf16 a[8:11], a[248:251], v[136:139], a[8:11]\n\t"
"v_mfma_f32_16x16x32_bf16 v[64:67], a[252:255], v[136:139], v[64:67]\n\t"
"s_xor_b32 s35, s34, 2\n\t"
"s_mul_i32 s35, s35, 40960\n\t"
"v_mfma_f32_16x16x32_bf16 v[64:67], a[236:239], v[132:135], v[64:67]\n\t"
"v_add_u32 v20, s35, v2\n\t"
"v_mfma_f32_16x16x32_bf16 v[68:71], a[236:239], v[140:143], v[68:71]\n\t"
"s_xor_b32 s35, s34, 1\n\t"
"s_mul_i32 s35, s35, 40960\n\t"
"v_mfma_f32_16x16x32_bf16 v[68:71], a[252:255], v[144:147], v[68:71]\n\t"
"buffer_load_dwordx4 v5, s[12:15], s42 offen offset:1024 lds\n\t"
"v_mfma_f32_16x16x32_bf16 a[20:23], a[248:251], v[144:147], a[20:23]\n\t"
"v_mfma_f32_16x16x32_bf16 a[20:23], a[232:235], v[140:143], a[20:23]\n\t"
"v_mov_b32_dpp v212, v100 row_shr:11 row_mask:0xf bank_mask:0x8\n\t"
"v_mfma_f32_16x16x32_bf16 a[16:19], a[228:231], v[140:143], a[16:19]\n\t"
"v_mfma_f32_16x16x32_bf16 a[16:19], a[244:247], v[144:147], a[16:19]\n\t"
"v_mov_b32_dpp v213, v101 row_shr:11 row_mask:0xf bank_mask:0x8\n\t"
"v_mfma_f32_16x16x32_bf16 a[12:15], a[240:243], v[144:147], a[12:15]\n\t"
"v_mfma_f32_16x16x32_bf16 a[12:15], a[224:227], v[140:143], a[12:15]\n\t"
"v_mov_b32_dpp v214, v102 row_shr:11 row_mask:0xf bank_mask:0x8\n\t"
"v_mfma_f32_16x16x32_bf16 a[24:27], a[224:227], v[164:167], a[24:27]\n\t"
"v_mfma_f32_16x16x32_bf16 a[24:27], a[240:243], v[168:171], a[24:27]\n\t"
"buffer_load_dwordx4 v6, s[12:15], s42 offen offset:2048 lds\n\t"
"v_mfma_f32_16x16x32_bf16 a[28:31], a[244:247], v[168:171], a[28:31]\n\t"
"v_mfma_f32_16x16x32_bf16 a[28:31], a[228:231], v[164:167], a[28:31]\n\t"
"v_mov_b32_dpp v215, v103 row_shr:11 row_mask:0xf bank_mask:0x8\n\t"
"v_mfma_f32_16x16x32_bf16 a[32:35], a[232:235], v[164:167], a[32:35]\n\t"
"v_mfma_f32_16x16x32_bf16 a[32:35], a[248:251], v[168:171], a[32:35]\n\t"
"v_mfma_f32_16x16x32_bf16 v[72:75], a[252:255], v[168:171], v[72:75]\n\t"
"v_mfma_f32_16x16x32_bf16 v[72:75], a[236:239], v[164:167], v[72:75]\n\t"
"v_mov_b32_dpp v212, v132 row_shl:1 row_mask:0xf bank_mask:0xf\n\t"
"v_mfma_f32_16x16x32_bf16 v[76:79], a[236:239], v[172:175], v[76:79]\n\t"
"v_mfma_f32_16x16x32_bf16 v[76:79], a[252:255], v[176:179], v[76:79]\n\t"
"buffer_load_dwordx4 v7, s[12:15], s42 offen offset:3072 lds\n\t"
"v_mfma_f32_16x16x32_bf16 a[44:47], a[248:251], v[176:179], a[44:47]\n\t"
"v_mov_b32_dpp v213, v133 row_shl:1 row_mask:0xf bank_mask:0xf\n\t"
"v_mfma_f32_16x16x32_bf16 a[44:47], a[232:235], v[172:175], a[44:47]\n\t"
"v_mov_b32_dpp v214, v134 row_shl:1 row_mask:0xf bank_mask:0xf\n\t"
"v_mfma_f32_16x16x32_bf16 a[40:43], a[228:231], v[172:175], a[40:43]\n\t"
"v_mov_b32_dpp v215, v135 row_shl:1 row_mask:0xf bank_mask:0xf\n\t"
"v_mfma_f32_16x16x32_bf16 a[40:43], a[244:247], v[176:179], a[40:43]\n\t"
"v_mfma_f32_16x16x32_bf16 a[36:39], a[240:243], v[176:179], a[36:39]\n\t"
"v_add_u32 v21, s35, v2\n\t"
"v_mfma_f32_16x16x32_bf16 a[36:39], a[224:227], v[172:175], a[36:39]\n\t"
"s_lshl_b32 s35, s39, 3\n\t"
"s_add_u32 s35, s35, s37\n\t"
"v_mfma_f32_16x16x32_bf16 a[48:51], a[224:227], v[180:183], a[48:51]\n\t"
"s_add_u32 m0, s26, 28672\n\t"
"s_add_u32 s40, s25, 6324224\n\t"
"v_mfma_f32_16x16x32_bf16 a[48:51], a[240:243], v[184:187], a[48:51]\n\t"
"buffer_load_dwordx4 v4, s[12:15], s40 offen lds\n\t"
"v_mfma_f32_16x16x32_bf16 a[52:55], a[244:247], v[184:187], a[52:55]\n\t"
"s_waitcnt vmcnt(11)\n\t"
"ds_read_b64_tr_b16 a[96:97], v12 offset:16384\n\t"
"s_lshl_b32 s35, s35, 2\n\t"
"s_add_u32 s35, s35, s34\n\t"
"v_mfma_f32_16x16x32_bf16 a[52:55], a[228:231], v[180:183], a[52:55]\n\t"
"ds_read_b64_tr_b16 a[98:99], v12 offset:17408\n\t"
"s_mul_i32 s35, s35, 4352\n\t"
"s_mov_b32 s18, 4352\n\t"
"v_mfma_f32_16x16x32_bf16 a[56:59], a[232:235], v[180:183], a[56:59]\n\t"
"ds_read_b64_tr_b16 a[100:101], v13 offset:16384\n\t"
"s_add_u32 s16, s16, s35\n\t"
"s_addc_u32 s17, s17, 0\n\t"
"v_mfma_f32_16x16x32_bf16 a[56:59], a[248:251], v[184:187], a[56:59]\n\t"
"buffer_load_dwordx4 v5, s[12:15], s40 offen offset:1024 lds\n\t"
"v_mfma_f32_16x16x32_bf16 v[80:83], a[252:255], v[184:187], v[80:83]\n\t"
"ds_read_b64_tr_b16 a[102:103], v13 offset:17408\n\t"
"s_mov_b32 s19, 0x20000\n\t"
"s_mul_i32 s35, s39, 2040832\n\t"
"v_mfma_f32_16x16x32_bf16 v[80:83], a[236:239], v[180:183], v[80:83]\n\t"
"ds_read_b64_tr_b16 a[104:105], v14 offset:16384\n\t"
"s_add_u32 s20, s20, s35\n\t"
"s_addc_u32 s21, s21, 0\n\t"
"v_mfma_f32_16x16x32_bf16 v[84:87], a[236:239], v[188:191], v[84:87]\n\t"
"ds_read_b64_tr_b16 a[106:107], v14 offset:17408\n\t"
"s_mov_b32 s22, 2040832\n\t"
"s_mov_b32 s23, 0x20000\n\t"
"v_mfma_f32_16x16x32_bf16 v[84:87], a[252:255], v[196:199], v[84:87]\n\t"
"buffer_load_dwordx4 v6, s[12:15], s40 offen offset:2048 lds\n\t"
"v_mfma_f32_16x16x32_bf16 a[68:71], a[248:251], v[196:199], a[68:71]\n\t"
"ds_read_b64_tr_b16 a[108:109], v15 offset:16384\n\t"
"v_mfma_f32_16x16x32_bf16 a[68:71], a[232:235], v[188:191], a[68:71]\n\t"
"ds_read_b64_tr_b16 a[110:111], v15 offset:17408\n\t"
"v_mov_b32_dpp v216, v104 row_shr:11 row_mask:0xf bank_mask:0x8\n\t"
"v_mfma_f32_16x16x32_bf16 a[64:67], a[228:231], v[188:191], a[64:67]\n\t"
"s_waitcnt vmcnt(7)\n\t"
"ds_read_b64_tr_b16 a[112:113], v12 offset:32768\n\t"
"v_mfma_f32_16x16x32_bf16 a[64:67], a[244:247], v[196:199], a[64:67]\n\t"
"buffer_load_dwordx4 v7, s[12:15], s40 offen offset:3072 lds\n\t"
"v_mov_b32_dpp v217, v105 row_shr:11 row_mask:0xf bank_mask:0x8\n\t"
"v_mfma_f32_16x16x32_bf16 a[60:63], a[240:243], v[196:199], a[60:63]\n\t"
"ds_read_b64_tr_b16 a[114:115], v12 offset:33792\n\t"
"v_mfma_f32_16x16x32_bf16 a[60:63], a[224:227], v[188:191], a[60:63]\n\t"
"v_mov_b32_dpp v218, v106 row_shr:11 row_mask:0xf bank_mask:0x8\n\t"
"v_mfma_f32_16x16x32_bf16 a[72:75], a[224:227], v[192:195], a[72:75]\n\t"
"s_add_u32 m0, s26, 12288\n\t"
"s_add_u32 s41, s25, 7340032\n\t"
"ds_read_b64_tr_b16 a[116:117], v13 offset:32768\n\t"
"v_mfma_f32_16x16x32_bf16 a[72:75], a[240:243], v[200:203], a[72:75]\n\t"
"buffer_load_dwordx4 v4, s[12:15], s41 offen lds\n\t"
"v_mfma_f32_16x16x32_bf16 a[76:79], a[244:247], v[200:203], a[76:79]\n\t"
"ds_read_b64_tr_b16 a[118:119], v13 offset:33792\n\t"
"v_mfma_f32_16x16x32_bf16 a[76:79], a[228:231], v[192:195], a[76:79]\n\t"
"v_mov_b32_dpp v219, v107 row_shr:11 row_mask:0xf bank_mask:0x8\n\t"
"v_mfma_f32_16x16x32_bf16 a[80:83], a[232:235], v[192:195], a[80:83]\n\t"
"ds_read_b64_tr_b16 a[120:121], v14 offset:32768\n\t"
"v_mfma_f32_16x16x32_bf16 a[80:83], a[248:251], v[200:203], a[80:83]\n\t"
"buffer_load_dwordx4 v5, s[12:15], s41 offen offset:1024 lds\n\t"
"v_mfma_f32_16x16x32_bf16 v[88:91], a[252:255], v[200:203], v[88:91]\n\t"
"ds_read_b64_tr_b16 a[122:123], v14 offset:33792\n\t"
"v_mfma_f32_16x16x32_bf16 v[88:91], a[236:239], v[192:195], v[88:91]\n\t"
"ds_read_b64_tr_b16 a[124:125], v15 offset:32768\n\t"
"v_mov_b32_dpp v216, v136 row_shl:1 row_mask:0xf bank_mask:0xf\n\t"
"v_mfma_f32_16x16x32_bf16 v[92:95], a[236:239], v[204:207], v[92:95]\n\t"
"ds_read_b64_tr_b16 a[126:127], v15 offset:33792\n\t"
"v_mfma_f32_16x16x32_bf16 v[92:95], a[252:255], v[208:211], v[92:95]\n\t"
"buffer_load_dwordx4 v6, s[12:15], s41 offen offset:2048 lds\n\t"
"v_mfma_f32_16x16x32_bf16 a[92:95], a[248:251], v[208:211], a[92:95]\n\t"
"v_mov_b32_dpp v217, v137 row_shl:1 row_mask:0xf bank_mask:0xf\n\t"
"v_mfma_f32_16x16x32_bf16 a[92:95], a[232:235], v[204:207], a[92:95]\n\t"
"v_mov_b32_dpp v218, v138 row_shl:1 row_mask:0xf bank_mask:0xf\n\t"
"v_mfma_f32_16x16x32_bf16 a[88:91], a[228:231], v[204:207], a[88:91]\n\t"
"v_mov_b32_dpp v219, v139 row_shl:1 row_mask:0xf bank_mask:0xf\n\t"
"v_mfma_f32_16x16x32_bf16 a[88:91], a[244:247], v[208:211], a[88:91]\n\t"
"buffer_load_dwordx4 v7, s[12:15], s41 offen offset:3072 lds\n\t"
"v_mfma_f32_16x16x32_bf16 a[84:87], a[240:243], v[208:211], a[84:87]\n\t"
"v_mov_b32 v30, v2\n\t"
"v_mfma_f32_16x16x32_bf16 a[84:87], a[224:227], v[204:207], a[84:87]\n\t"
"s_waitcnt lgkmcnt(0)\n\t"
"v_and_b32 v2, 15, v1\n\t"
"v_mfma_f32_16x16x32_bf16 a[0:3], a[96:99], v[140:143], a[0:3]\n\t"
"s_add_u32 m0, s26, 24576\n\t"
"s_add_u32 s42, s7, 0\n\t"
"v_mfma_f32_16x16x32_bf16 a[0:3], a[112:115], v[144:147], a[0:3]\n\t"
"buffer_load_dwordx4 v8, s[8:11], s42 offen lds\n\t"
"v_mfma_f32_16x16x32_bf16 a[4:7], a[116:119], v[144:147], a[4:7]\n\t"
"v_and_b32 v3, 0xffe0, v25\n\t"
"v_mfma_f32_16x16x32_bf16 a[4:7], a[100:103], v[140:143], a[4:7]\n\t"
"v_lshl_add_u32 v3, v2, 1, v3\n\t"
"v_mfma_f32_16x16x32_bf16 a[8:11], a[104:107], v[140:143], a[8:11]\n\t"
"v_mov_b32 v27, 0\n\t"
"v_mfma_f32_16x16x32_bf16 a[8:11], a[120:123], v[144:147], a[8:11]\n\t"
"buffer_load_dwordx4 v9, s[8:11], s42 offen offset:1024 lds\n\t"
"v_mfma_f32_16x16x32_bf16 v[64:67], a[124:127], v[144:147], v[64:67]\n\t"
"v_mov_b32 v28, 0\n\t"
"v_mfma_f32_16x16x32_bf16 v[64:67], a[108:111], v[140:143], v[64:67]\n\t"
"s_lshl_b32 s35, s38, 8\n\t"
"s_add_u32 s35, s35, 256\n\t"
"v_mfma_f32_16x16x32_bf16 v[68:71], a[108:111], v[164:167], v[68:71]\n\t"
"v_and_b32 v29, 60, v1\n\t"
"v_mfma_f32_16x16x32_bf16 v[68:71], a[124:127], v[168:171], v[68:71]\n\t"
"buffer_load_dwordx4 v8, s[8:11], s42 offen offset:2048 lds\n\t"
"v_mfma_f32_16x16x32_bf16 a[20:23], a[120:123], v[168:171], a[20:23]\n\t"
"v_mfma_f32_16x16x32_bf16 a[20:23], a[104:107], v[164:167], a[20:23]\n\t"
"v_mov_b32_dpp v220, v100 row_shr:10 row_mask:0xf bank_mask:0x8\n\t"
"v_mfma_f32_16x16x32_bf16 a[16:19], a[100:103], v[164:167], a[16:19]\n\t"
"v_mfma_f32_16x16x32_bf16 a[16:19], a[116:119], v[168:171], a[16:19]\n\t"
"buffer_load_dwordx4 v9, s[8:11], s42 offen offset:3072 lds\n\t"
"v_mov_b32_dpp v221, v101 row_shr:10 row_mask:0xf bank_mask:0x8\n\t"
"v_mfma_f32_16x16x32_bf16 a[12:15], a[112:115], v[168:171], a[12:15]\n\t"
"v_mfma_f32_16x16x32_bf16 a[12:15], a[96:99], v[164:167], a[12:15]\n\t"
"v_mov_b32_dpp v222, v102 row_shr:10 row_mask:0xf bank_mask:0x8\n\t"
"v_mfma_f32_16x16x32_bf16 a[24:27], a[96:99], v[172:175], a[24:27]\n\t"
"s_add_u32 m0, s26, 0\n\t"
"s_add_u32 s40, s7, 4096\n\t"
"v_mfma_f32_16x16x32_bf16 a[24:27], a[112:115], v[176:179], a[24:27]\n\t"
"buffer_load_dwordx4 v8, s[8:11], s40 offen lds\n\t"
"v_mfma_f32_16x16x32_bf16 a[28:31], a[116:119], v[176:179], a[28:31]\n\t"
"v_mfma_f32_16x16x32_bf16 a[28:31], a[100:103], v[172:175], a[28:31]\n\t"
"v_mov_b32_dpp v223, v103 row_shr:10 row_mask:0xf bank_mask:0x8\n\t"
"v_mfma_f32_16x16x32_bf16 a[32:35], a[104:107], v[172:175], a[32:35]\n\t"
"v_mfma_f32_16x16x32_bf16 a[32:35], a[120:123], v[176:179], a[32:35]\n\t"
"buffer_load_dwordx4 v9, s[8:11], s40 offen offset:1024 lds\n\t"
"v_mfma_f32_16x16x32_bf16 v[72:75], a[124:127], v[176:179], v[72:75]\n\t"
"v_mfma_f32_16x16x32_bf16 v[72:75], a[108:111], v[172:175], v[72:75]\n\t"
"v_mov_b32_dpp v220, v140 row_shl:1 row_mask:0xf bank_mask:0xf\n\t"
"v_mfma_f32_16x16x32_bf16 v[76:79], a[108:111], v[180:183], v[76:79]\n\t"
"v_mfma_f32_16x16x32_bf16 v[76:79], a[124:127], v[184:187], v[76:79]\n\t"
"buffer_load_dwordx4 v8, s[8:11], s40 offen offset:2048 lds\n\t"
"v_mfma_f32_16x16x32_bf16 a[44:47], a[120:123], v[184:187], a[44:47]\n\t"
"v_mov_b32_dpp v221, v141 row_shl:1 row_mask:0xf bank_mask:0xf\n\t"
"v_mfma_f32_16x16x32_bf16 a[44:47], a[104:107], v[180:183], a[44:47]\n\t"
"v_mov_b32_dpp v222, v142 row_shl:1 row_mask:0xf bank_mask:0xf\n\t"
"v_mfma_f32_16x16x32_bf16 a[40:43], a[100:103], v[180:183], a[40:43]\n\t"
"v_mov_b32_dpp v223, v143 row_shl:1 row_mask:0xf bank_mask:0xf\n\t"
"v_mfma_f32_16x16x32_bf16 a[40:43], a[116:119], v[184:187], a[40:43]\n\t"
"buffer_load_dwordx4 v9, s[8:11], s40 offen offset:3072 lds\n\t"
"v_mfma_f32_16x16x32_bf16 a[36:39], a[112:115], v[184:187], a[36:39]\n\t"
"v_lshl_add_u32 v29, v29, 2, s35\n\t"
"v_mfma_f32_16x16x32_bf16 a[36:39], a[96:99], v[180:183], a[36:39]\n\t"
"v_mfma_f32_16x16x32_bf16 a[48:51], a[96:99], v[188:191], a[48:51]\n\t"
"s_add_u32 m0, s26, 4096\n\t"
"s_add_u32 s41, s7, 8192\n\t"
"v_mfma_f32_16x16x32_bf16 a[48:51], a[112:115], v[196:199], a[48:51]\n\t"
"buffer_load_dwordx4 v8, s[8:11], s41 offen lds\n\t"
"v_mfma_f32_16x16x32_bf16 a[52:55], a[116:119], v[196:199], a[52:55]\n\t"
"s_waitcnt vmcnt(17)\n\t"
"ds_read_b64_tr_b16 a[128:129], v12 offset:36864\n\t"
"v_mfma_f32_16x16x32_bf16 a[52:55], a[100:103], v[188:191], a[52:55]\n\t"
"ds_read_b64_tr_b16 a[130:131], v12 offset:37888\n\t"
"v_mfma_f32_16x16x32_bf16 a[56:59], a[104:107], v[188:191], a[56:59]\n\t"
"buffer_load_dwordx4 v9, s[8:11], s41 offen offset:1024 lds\n\t"
"ds_read_b64_tr_b16 a[132:133], v13 offset:36864\n\t"
"v_mfma_f32_16x16x32_bf16 a[56:59], a[120:123], v[196:199], a[56:59]\n\t"
"v_mfma_f32_16x16x32_bf16 v[80:83], a[124:127], v[196:199], v[80:83]\n\t"
"ds_read_b64_tr_b16 a[134:135], v13 offset:37888\n\t"
"v_mfma_f32_16x16x32_bf16 v[80:83], a[108:111], v[188:191], v[80:83]\n\t"
"buffer_load_dwordx4 v8, s[8:11], s41 offen offset:2048 lds\n\t"
"ds_read_b64_tr_b16 a[136:137], v14 offset:36864\n\t"
"v_mfma_f32_16x16x32_bf16 v[84:87], a[108:111], v[192:195], v[84:87]\n\t"
"ds_read_b64_tr_b16 a[138:139], v14 offset:37888\n\t"
"v_mfma_f32_16x16x32_bf16 v[84:87], a[124:127], v[200:203], v[84:87]\n\t"
"v_mfma_f32_16x16x32_bf16 a[68:71], a[120:123], v[200:203], a[68:71]\n\t"
"buffer_load_dwordx4 v9, s[8:11], s41 offen offset:3072 lds\n\t"
"ds_read_b64_tr_b16 a[140:141], v15 offset:36864\n\t"
"v_mfma_f32_16x16x32_bf16 a[68:71], a[104:107], v[192:195], a[68:71]\n\t"
"ds_read_b64_tr_b16 a[142:143], v15 offset:37888\n\t"
"v_mov_b32_dpp v224, v104 row_shr:10 row_mask:0xf bank_mask:0x8\n\t"
"v_mfma_f32_16x16x32_bf16 a[64:67], a[100:103], v[192:195], a[64:67]\n\t"
"s_waitcnt vmcnt(16)\n\t"
"ds_read_b64_tr_b16 a[144:145], v12 offset:28672\n\t"
"v_mfma_f32_16x16x32_bf16 a[64:67], a[116:119], v[200:203], a[64:67]\n\t"
"s_add_u32 m0, s26, 20480\n\t"
"s_add_u32 s42, s7, 12288\n\t"
"v_mov_b32_dpp v225, v105 row_shr:10 row_mask:0xf bank_mask:0x8\n\t"
"v_mfma_f32_16x16x32_bf16 a[60:63], a[112:115], v[200:203], a[60:63]\n\t"
"buffer_load_dwordx4 v8, s[8:11], s42 offen lds\n\t"
"ds_read_b64_tr_b16 a[146:147], v12 offset:29696\n\t"
"v_mfma_f32_16x16x32_bf16 a[60:63], a[96:99], v[192:195], a[60:63]\n\t"
"v_mov_b32_dpp v226, v106 row_shr:10 row_mask:0xf bank_mask:0x8\n\t"
"v_mfma_f32_16x16x32_bf16 a[72:75], a[96:99], v[204:207], a[72:75]\n\t"
"ds_read_b64_tr_b16 a[148:149], v13 offset:28672\n\t"
"v_mfma_f32_16x16x32_bf16 a[72:75], a[112:115], v[208:211], a[72:75]\n\t"
"buffer_load_dwordx4 v9, s[8:11], s42 offen offset:1024 lds\n\t"
"v_mfma_f32_16x16x32_bf16 a[76:79], a[116:119], v[208:211], a[76:79]\n\t"
"ds_read_b64_tr_b16 a[150:151], v13 offset:29696\n\t"
"v_mfma_f32_16x16x32_bf16 a[76:79], a[100:103], v[204:207], a[76:79]\n\t"
"v_mov_b32_dpp v227, v107 row_shr:10 row_mask:0xf bank_mask:0x8\n\t"
"v_mfma_f32_16x16x32_bf16 a[80:83], a[104:107], v[204:207], a[80:83]\n\t"
"buffer_load_dwordx4 v8, s[8:11], s42 offen offset:2048 lds\n\t"
"ds_read_b64_tr_b16 a[152:153], v14 offset:28672\n\t"
"v_mfma_f32_16x16x32_bf16 a[80:83], a[120:123], v[208:211], a[80:83]\n\t"
"v_mfma_f32_16x16x32_bf16 v[88:91], a[124:127], v[208:211], v[88:91]\n\t"
"ds_read_b64_tr_b16 a[154:155], v14 offset:29696\n\t"
"v_mfma_f32_16x16x32_bf16 v[88:91], a[108:111], v[204:207], v[88:91]\n\t"
"buffer_load_dwordx4 v9, s[8:11], s42 offen offset:3072 lds\n\t"
"ds_read_b64_tr_b16 a[156:157], v15 offset:28672\n\t"
"v_mov_b32_dpp v224, v144 row_shl:1 row_mask:0xf bank_mask:0xf\n\t"
"v_mfma_f32_16x16x32_bf16 v[92:95], a[108:111], v[212:215], v[92:95]\n\t"
"ds_read_b64_tr_b16 a[158:159], v15 offset:29696\n\t"
"v_mfma_f32_16x16x32_bf16 v[92:95], a[124:127], v[216:219], v[92:95]\n\t"
"v_mfma_f32_16x16x32_bf16 a[92:95], a[120:123], v[216:219], a[92:95]\n\t"
"s_add_u32 m0, s26, 8192\n\t"
"s_add_u32 s40, s7, 262144\n\t"
"v_mov_b32_dpp v225, v145 row_shl:1 row_mask:0xf bank_mask:0xf\n\t"
"v_mfma_f32_16x16x32_bf16 a[92:95], a[104:107], v[212:215], a[92:95]\n\t"
"buffer_load_dwordx4 v10, s[8:11], s40 offen lds\n\t"
"v_mov_b32_dpp v226, v146 row_shl:1 row_mask:0xf bank_mask:0xf\n\t"
"v_mfma_f32_16x16x32_bf16 a[88:91], a[100:103], v[212:215], a[88:91]\n\t"
"v_mov_b32_dpp v227, v147 row_shl:1 row_mask:0xf bank_mask:0xf\n\t"
"v_mfma_f32_16x16x32_bf16 a[88:91], a[116:119], v[216:219], a[88:91]\n\t"
"v_mfma_f32_16x16x32_bf16 a[84:87], a[112:115], v[216:219], a[84:87]\n\t"
"v_mfma_f32_16x16x32_bf16 a[84:87], a[96:99], v[212:215], a[84:87]\n\t"
"s_waitcnt lgkmcnt(0)\n\t"
"s_waitcnt vmcnt(9)\n\t"
"ds_read_b128 v[244:247], v16 offset:0\n\t"
"v_mfma_f32_16x16x32_bf16 a[0:3], a[128:131], v[164:167], a[0:3]\n\t"
"s_add_u32 m0, s26, 16384\n\t"
"s_add_u32 s41, s25, 7372800\n\t"
"v_mfma_f32_16x16x32_bf16 a[0:3], a[144:147], v[168:171], a[0:3]\n\t"
"buffer_load_dwordx4 v4, s[12:15], s41 offen lds\n\t"
"v_mfma_f32_16x16x32_bf16 a[4:7], a[148:151], v[168:171], a[4:7]\n\t"
"ds_read_b128 v[228:231], v16 offset:24576\n\t"
"v_mfma_f32_16x16x32_bf16 a[4:7], a[132:135], v[164:167], a[4:7]\n\t"
"ds_read_b128 v[248:251], v17 offset:0\n\t"
"v_mfma_f32_16x16x32_bf16 a[8:11], a[136:139], v[164:167], a[8:11]\n\t"
"ds_read_b128 v[232:235], v17 offset:24576\n\t"
"v_mfma_f32_16x16x32_bf16 a[8:11], a[152:155], v[168:171], a[8:11]\n\t"
"buffer_load_dwordx4 v5, s[12:15], s41 offen offset:1024 lds\n\t"
"ds_read_b128 v[252:255], v16 offset:2048\n\t"
"v_mfma_f32_16x16x32_bf16 v[64:67], a[156:159], v[168:171], v[64:67]\n\t"
"ds_read_b128 v[236:239], v16 offset:26624\n\t"
"v_mfma_f32_16x16x32_bf16 v[64:67], a[140:143], v[164:167], v[64:67]\n\t"
"ds_read_b128 v[40:43], v17 offset:2048\n\t"
"v_mfma_f32_16x16x32_bf16 v[68:71], a[140:143], v[172:175], v[68:71]\n\t"
"ds_read_b128 v[240:243], v17 offset:26624\n\t"
"v_mfma_f32_16x16x32_bf16 v[68:71], a[156:159], v[176:179], v[68:71]\n\t"
"buffer_load_dwordx4 v6, s[12:15], s41 offen offset:2048 lds\n\t"
"v_mfma_f32_16x16x32_bf16 a[20:23], a[152:155], v[176:179], a[20:23]\n\t"
"v_mfma_f32_16x16x32_bf16 a[20:23], a[136:139], v[172:175], a[20:23]\n\t"
"v_mov_b32_dpp v44, v100 row_shr:9 row_mask:0xf bank_mask:0x8\n\t"
"v_mfma_f32_16x16x32_bf16 a[16:19], a[132:135], v[172:175], a[16:19]\n\t"
"v_mfma_f32_16x16x32_bf16 a[16:19], a[148:151], v[176:179], a[16:19]\n\t"
"buffer_load_dwordx4 v7, s[12:15], s41 offen offset:3072 lds\n\t"
"v_mov_b32_dpp v45, v101 row_shr:9 row_mask:0xf bank_mask:0x8\n\t"
"v_mfma_f32_16x16x32_bf16 a[12:15], a[144:147], v[176:179], a[12:15]\n\t"
"v_mfma_f32_16x16x32_bf16 a[12:15], a[128:131], v[172:175], a[12:15]\n\t"
"v_mov_b32_dpp v46, v102 row_shr:9 row_mask:0xf bank_mask:0x8\n\t"
"v_mfma_f32_16x16x32_bf16 a[24:27], a[128:131], v[180:183], a[24:27]\n\t"
"s_add_u32 m0, s26, 32768\n\t"
"s_add_u32 s42, s6, 0\n\t"
"v_mfma_f32_16x16x32_bf16 a[24:27], a[144:147], v[184:187], a[24:27]\n\t"
"buffer_load_dwordx4 v4, s[12:15], s42 offen lds\n\t"
"v_mfma_f32_16x16x32_bf16 a[28:31], a[148:151], v[184:187], a[28:31]\n\t"
"v_mfma_f32_16x16x32_bf16 a[28:31], a[132:135], v[180:183], a[28:31]\n\t"
"v_mov_b32_dpp v47, v103 row_shr:9 row_mask:0xf bank_mask:0x8\n\t"
"v_mfma_f32_16x16x32_bf16 a[32:35], a[136:139], v[180:183], a[32:35]\n\t"
"v_mfma_f32_16x16x32_bf16 a[32:35], a[152:155], v[184:187], a[32:35]\n\t"
"buffer_load_dwordx4 v5, s[12:15], s42 offen offset:1024 lds\n\t"
"v_mfma_f32_16x16x32_bf16 v[72:75], a[156:159], v[184:187], v[72:75]\n\t"
"v_mfma_f32_16x16x32_bf16 v[72:75], a[140:143], v[180:183], v[72:75]\n\t"
"v_mov_b32_dpp v44, v164 row_shl:1 row_mask:0xf bank_mask:0xf\n\t"
"v_mfma_f32_16x16x32_bf16 v[76:79], a[140:143], v[188:191], v[76:79]\n\t"
"v_mfma_f32_16x16x32_bf16 v[76:79], a[156:159], v[196:199], v[76:79]\n\t"
"buffer_load_dwordx4 v6, s[12:15], s42 offen offset:2048 lds\n\t"
"v_mfma_f32_16x16x32_bf16 a[44:47], a[152:155], v[196:199], a[44:47]\n\t"
"v_mov_b32_dpp v45, v165 row_shl:1 row_mask:0xf bank_mask:0xf\n\t"
"v_mfma_f32_16x16x32_bf16 a[44:47], a[136:139], v[188:191], a[44:47]\n\t"
"v_mov_b32_dpp v46, v166 row_shl:1 row_mask:0xf bank_mask:0xf\n\t"
"v_mfma_f32_16x16x32_bf16 a[40:43], a[132:135], v[188:191], a[40:43]\n\t"
"v_mov_b32_dpp v47, v167 row_shl:1 row_mask:0xf bank_mask:0xf\n\t"
"v_mfma_f32_16x16x32_bf16 a[40:43], a[148:151], v[196:199], a[40:43]\n\t"
"buffer_load_dwordx4 v7, s[12:15], s42 offen offset:3072 lds\n\t"
"v_mfma_f32_16x16x32_bf16 a[36:39], a[144:147], v[196:199], a[36:39]\n\t"
"v_mfma_f32_16x16x32_bf16 a[36:39], a[128:131], v[188:191], a[36:39]\n\t"
"s_waitcnt vmcnt(9)\n\t"
"ds_read_b128 v[116:119], v16 offset:20480\n\t"
"v_mfma_f32_16x16x32_bf16 a[48:51], a[128:131], v[192:195], a[48:51]\n\t"
"s_add_u32 m0, s26, 36864\n\t"
"s_add_u32 s40, s6, 32768\n\t"
"v_mfma_f32_16x16x32_bf16 a[48:51], a[144:147], v[200:203], a[48:51]\n\t"
"buffer_load_dwordx4 v4, s[12:15], s40 offen lds\n\t"
"v_mfma_f32_16x16x32_bf16 a[52:55], a[148:151], v[200:203], a[52:55]\n\t"
"ds_read_b64_tr_b16 a[160:161], v12 offset:12288\n\t"
"ds_read_b128 v[48:51], v16 offset:4096\n\t"
"v_mfma_f32_16x16x32_bf16 a[52:55], a[132:135], v[192:195], a[52:55]\n\t"
"ds_read_b64_tr_b16 a[162:163], v12 offset:13312\n\t"
"ds_read_b128 v[120:123], v17 offset:20480\n\t"
"v_mfma_f32_16x16x32_bf16 a[56:59], a[136:139], v[192:195], a[56:59]\n\t"
"ds_read_b64_tr_b16 a[164:165], v13 offset:12288\n\t"
"ds_read_b128 v[52:55], v17 offset:4096\n\t"
"v_mfma_f32_16x16x32_bf16 a[56:59], a[152:155], v[200:203], a[56:59]\n\t"
"buffer_load_dwordx4 v5, s[12:15], s40 offen offset:1024 lds\n\t"
"ds_read_b128 v[124:127], v16 offset:22528\n\t"
"v_mfma_f32_16x16x32_bf16 v[80:83], a[156:159], v[200:203], v[80:83]\n\t"
"ds_read_b64_tr_b16 a[166:167], v13 offset:13312\n\t"
"ds_read_b128 v[56:59], v16 offset:6144\n\t"
"v_mfma_f32_16x16x32_bf16 v[80:83], a[140:143], v[192:195], v[80:83]\n\t"
"ds_read_b64_tr_b16 a[168:169], v14 offset:12288\n\t"
"ds_read_b128 v[148:151], v17 offset:22528\n\t"
"v_mfma_f32_16x16x32_bf16 v[84:87], a[140:143], v[204:207], v[84:87]\n\t"
"ds_read_b64_tr_b16 a[170:171], v14 offset:13312\n\t"
"ds_read_b128 v[60:63], v17 offset:6144\n\t"
"v_mfma_f32_16x16x32_bf16 v[84:87], a[156:159], v[208:211], v[84:87]\n\t"
"buffer_load_dwordx4 v6, s[12:15], s40 offen offset:2048 lds\n\t"
"s_waitcnt vmcnt(11)\n\t"
"ds_read_b128 v[108:111], v16 offset:8192\n\t"
"v_mfma_f32_16x16x32_bf16 a[68:71], a[152:155], v[208:211], a[68:71]\n\t"
"ds_read_b64_tr_b16 a[172:173], v15 offset:12288\n\t"
"v_mfma_f32_16x16x32_bf16 a[68:71], a[136:139], v[204:207], a[68:71]\n\t"
"ds_read_b64_tr_b16 a[174:175], v15 offset:13312\n\t"
"ds_read_b128 v[112:115], v17 offset:8192\n\t"
"v_mov_b32_dpp v128, v104 row_shr:9 row_mask:0xf bank_mask:0x8\n\t"
"v_mfma_f32_16x16x32_bf16 a[64:67], a[132:135], v[204:207], a[64:67]\n\t"
"s_waitcnt vmcnt(7)\n\t"
"ds_read_b64_tr_b16 a[176:177], v12 offset:16384\n\t"
"v_mfma_f32_16x16x32_bf16 a[64:67], a[148:151], v[208:211], a[64:67]\n\t"
"buffer_load_dwordx4 v7, s[12:15], s40 offen offset:3072 lds\n\t"
"v_mov_b32_dpp v129, v105 row_shr:9 row_mask:0xf bank_mask:0x8\n\t"
"v_mfma_f32_16x16x32_bf16 a[60:63], a[144:147], v[208:211], a[60:63]\n\t"
"ds_read_b64_tr_b16 a[178:179], v12 offset:17408\n\t"
"v_mfma_f32_16x16x32_bf16 a[60:63], a[128:131], v[204:207], a[60:63]\n\t"
"v_mov_b32_dpp v130, v106 row_shr:9 row_mask:0xf bank_mask:0x8\n\t"
"v_mfma_f32_16x16x32_bf16 a[72:75], a[128:131], v[212:215], a[72:75]\n\t"
"s_add_u32 m0, s26, 28672\n\t"
"s_add_u32 s41, s6, 1048576\n\t"
"ds_read_b64_tr_b16 a[180:181], v13 offset:16384\n\t"
"v_mfma_f32_16x16x32_bf16 a[72:75], a[144:147], v[216:219], a[72:75]\n\t"
"buffer_load_dwordx4 v4, s[12:15], s41 offen lds\n\t"
"v_mfma_f32_16x16x32_bf16 a[76:79], a[148:151], v[216:219], a[76:79]\n\t"
"ds_read_b64_tr_b16 a[182:183], v13 offset:17408\n\t"
"v_mfma_f32_16x16x32_bf16 a[76:79], a[132:135], v[212:215], a[76:79]\n\t"
"v_mov_b32_dpp v131, v107 row_shr:9 row_mask:0xf bank_mask:0x8\n\t"
"v_mfma_f32_16x16x32_bf16 a[80:83], a[136:139], v[212:215], a[80:83]\n\t"
"ds_read_b64_tr_b16 a[184:185], v14 offset:16384\n\t"
"v_mfma_f32_16x16x32_bf16 a[80:83], a[152:155], v[216:219], a[80:83]\n\t"
"buffer_load_dwordx4 v5, s[12:15], s41 offen offset:1024 lds\n\t"
"v_mfma_f32_16x16x32_bf16 v[88:91], a[156:159], v[216:219], v[88:91]\n\t"
"ds_read_b64_tr_b16 a[186:187], v14 offset:17408\n\t"
"v_mfma_f32_16x16x32_bf16 v[88:91], a[140:143], v[212:215], v[88:91]\n\t"
"ds_read_b64_tr_b16 a[188:189], v15 offset:16384\n\t"
"v_mov_b32_dpp v128, v168 row_shl:1 row_mask:0xf bank_mask:0xf\n\t"
"v_mfma_f32_16x16x32_bf16 v[92:95], a[140:143], v[220:223], v[92:95]\n\t"
"ds_read_b64_tr_b16 a[190:191], v15 offset:17408\n\t"
"v_mfma_f32_16x16x32_bf16 v[92:95], a[156:159], v[224:227], v[92:95]\n\t"
"buffer_load_dwordx4 v6, s[12:15], s41 offen offset:2048 lds\n\t"
"v_mfma_f32_16x16x32_bf16 a[92:95], a[152:155], v[224:227], a[92:95]\n\t"
"v_mov_b32_dpp v129, v169 row_shl:1 row_mask:0xf bank_mask:0xf\n\t"
"v_mfma_f32_16x16x32_bf16 a[92:95], a[136:139], v[220:223], a[92:95]\n\t"
"v_mov_b32_dpp v130, v170 row_shl:1 row_mask:0xf bank_mask:0xf\n\t"
"v_mfma_f32_16x16x32_bf16 a[88:91], a[132:135], v[220:223], a[88:91]\n\t"
"v_mov_b32_dpp v131, v171 row_shl:1 row_mask:0xf bank_mask:0xf\n\t"
"v_mfma_f32_16x16x32_bf16 a[88:91], a[148:151], v[224:227], a[88:91]\n\t"
"buffer_load_dwordx4 v7, s[12:15], s41 offen offset:3072 lds\n\t"
"v_mfma_f32_16x16x32_bf16 a[84:87], a[144:147], v[224:227], a[84:87]\n\t"
"v_mfma_f32_16x16x32_bf16 a[84:87], a[128:131], v[220:223], a[84:87]\n\t"
"s_xor_b32 s43, s96, 2162688\n\t"
"buffer_load_dword v24, v11, s[12:15], s43 offen\n\t"
"s_xor_b32 s43, s33, 4259840\n\t"
"buffer_load_dword v24, v22, s[12:15], s43 offen\n\t"
"s_waitcnt lgkmcnt(0)\n\t"
"v_cmp_lt_u32 vcc, 47, v1\n\t"
"v_mfma_f32_16x16x32_bf16 a[0:3], a[160:163], v[172:175], a[0:3]\n\t"
"v_mfma_f32_16x16x32_bf16 a[0:3], a[176:179], v[176:179], a[0:3]\n\t"
"v_mfma_f32_16x16x32_bf16 a[4:7], a[180:183], v[176:179], a[4:7]\n\t"
"v_mov_b32 v38, 0x3d838849\n\t"
"v_mfma_f32_16x16x32_bf16 a[4:7], a[164:167], v[172:175], a[4:7]\n\t"
"v_mov_b32 v39, 0x3d160773\n\t"
"v_mfma_f32_16x16x32_bf16 a[8:11], a[168:171], v[172:175], a[8:11]\n\t"
"s_lshl_b32 s35, s38, 17\n\t"
"v_mfma_f32_16x16x32_bf16 a[8:11], a[184:187], v[176:179], a[8:11]\n\t"
"v_mfma_f32_16x16x32_bf16 v[64:67], a[188:191], v[176:179], v[64:67]\n\t"
"v_cndmask_b32 v38, v38, v39, vcc\n\t"
"v_mfma_f32_16x16x32_bf16 v[64:67], a[172:175], v[172:175], v[64:67]\n\t"
"v_lshrrev_b32 v2, 3, v1\n\t"
"v_mfma_f32_16x16x32_bf16 v[68:71], a[172:175], v[180:183], v[68:71]\n\t"
"v_lshl_add_u32 v31, v2, 13, s35\n\t"
"v_mfma_f32_16x16x32_bf16 v[68:71], a[188:191], v[184:187], v[68:71]\n\t"
"v_mfma_f32_16x16x32_bf16 a[20:23], a[184:187], v[184:187], a[20:23]\n\t"
"v_mfma_f32_16x16x32_bf16 a[20:23], a[168:171], v[180:183], a[20:23]\n\t"
"v_mfma_f32_16x16x32_bf16 a[16:19], a[164:167], v[180:183], a[16:19]\n\t"
"v_mfma_f32_16x16x32_bf16 a[16:19], a[180:183], v[184:187], a[16:19]\n\t"
"v_mfma_f32_16x16x32_bf16 a[12:15], a[176:179], v[184:187], a[12:15]\n\t"
"v_mfma_f32_16x16x32_bf16 a[12:15], a[160:163], v[180:183], a[12:15]\n\t"
"v_mfma_f32_16x16x32_bf16 a[24:27], a[160:163], v[188:191], a[24:27]\n\t"
"v_mfma_f32_16x16x32_bf16 a[24:27], a[176:179], v[196:199], a[24:27]\n\t"
"v_mfma_f32_16x16x32_bf16 a[28:31], a[180:183], v[196:199], a[28:31]\n\t"
"v_mfma_f32_16x16x32_bf16 a[28:31], a[164:167], v[188:191], a[28:31]\n\t"
"v_mfma_f32_16x16x32_bf16 a[32:35], a[168:171], v[188:191], a[32:35]\n\t"
"v_mfma_f32_16x16x32_bf16 a[32:35], a[184:187], v[196:199], a[32:35]\n\t"
"v_mfma_f32_16x16x32_bf16 v[72:75], a[188:191], v[196:199], v[72:75]\n\t"
"v_mfma_f32_16x16x32_bf16 v[72:75], a[172:175], v[188:191], v[72:75]\n\t"
"v_mfma_f32_16x16x32_bf16 v[76:79], a[172:175], v[192:195], v[76:79]\n\t"
"v_mfma_f32_16x16x32_bf16 v[76:79], a[188:191], v[200:203], v[76:79]\n\t"
"v_mfma_f32_16x16x32_bf16 a[44:47], a[184:187], v[200:203], a[44:47]\n\t"
"v_mfma_f32_16x16x32_bf16 a[44:47], a[168:171], v[192:195], a[44:47]\n\t"
"v_mfma_f32_16x16x32_bf16 a[40:43], a[164:167], v[192:195], a[40:43]\n\t"
"v_mfma_f32_16x16x32_bf16 a[40:43], a[180:183], v[200:203], a[40:43]\n\t"
"v_mfma_f32_16x16x32_bf16 a[36:39], a[176:179], v[200:203], a[36:39]\n\t"
"v_and_b32 v2, 7, v1\n\t"
"v_mfma_f32_16x16x32_bf16 a[36:39], a[160:163], v[192:195], a[36:39]\n\t"
"v_lshl_or_b32 v31, v2, 4, v31\n\t"
"v_mfma_f32_16x16x32_bf16 a[48:51], a[160:163], v[204:207], a[48:51]\n\t"
"s_add_u32 m0, s26, 0\n\t"
"s_add_u32 s42, s6, 1081344\n\t"
"v_mfma_f32_16x16x32_bf16 a[48:51], a[176:179], v[208:211], a[48:51]\n\t"
"buffer_load_dwordx4 v4, s[12:15], s42 offen lds\n\t"
"v_mfma_f32_16x16x32_bf16 a[52:55], a[180:183], v[208:211], a[52:55]\n\t"
"s_waitcnt vmcnt(11)\n\t"
"ds_read_b64_tr_b16 a[192:193], v12 offset:32768\n\t"
"s_lshl4_add_u32 s35, s34, s37\n\t"
"s_lshl_b32 s35, s35, 7\n\t"
"v_mfma_f32_16x16x32_bf16 a[52:55], a[164:167], v[204:207], a[52:55]\n\t"
"ds_read_b64_tr_b16 a[194:195], v12 offset:33792\n\t"
"v_add_u32 v31, s35, v31\n\t"
"v_mfma_f32_16x16x32_bf16 a[56:59], a[168:171], v[204:207], a[56:59]\n\t"
"ds_read_b64_tr_b16 a[196:197], v13 offset:32768\n\t"
"v_mfma_f32_16x16x32_bf16 a[56:59], a[184:187], v[208:211], a[56:59]\n\t"
"buffer_load_dwordx4 v5, s[12:15], s42 offen offset:1024 lds\n\t"
"v_mfma_f32_16x16x32_bf16 v[80:83], a[188:191], v[208:211], v[80:83]\n\t"
"ds_read_b64_tr_b16 a[198:199], v13 offset:33792\n\t"
"v_mfma_f32_16x16x32_bf16 v[80:83], a[172:175], v[204:207], v[80:83]\n\t"
"ds_read_b64_tr_b16 a[200:201], v14 offset:32768\n\t"
"v_mfma_f32_16x16x32_bf16 v[84:87], a[172:175], v[212:215], v[84:87]\n\t"
"ds_read_b64_tr_b16 a[202:203], v14 offset:33792\n\t"
"v_mfma_f32_16x16x32_bf16 v[84:87], a[188:191], v[216:219], v[84:87]\n\t"
"buffer_load_dwordx4 v6, s[12:15], s42 offen offset:2048 lds\n\t"
"v_mfma_f32_16x16x32_bf16 a[68:71], a[184:187], v[216:219], a[68:71]\n\t"
"ds_read_b64_tr_b16 a[204:205], v15 offset:32768\n\t"
"v_mfma_f32_16x16x32_bf16 a[68:71], a[168:171], v[212:215], a[68:71]\n\t"
"ds_read_b64_tr_b16 a[206:207], v15 offset:33792\n\t"
"v_mfma_f32_16x16x32_bf16 a[64:67], a[164:167], v[212:215], a[64:67]\n\t"
"s_waitcnt vmcnt(9)\n\t"
"ds_read_b64_tr_b16 a[208:209], v12 offset:36864\n\t"
"v_mfma_f32_16x16x32_bf16 a[64:67], a[180:183], v[216:219], a[64:67]\n\t"
"buffer_load_dwordx4 v7, s[12:15], s42 offen offset:3072 lds\n\t"
"v_mfma_f32_16x16x32_bf16 a[60:63], a[176:179], v[216:219], a[60:63]\n\t"
"ds_read_b64_tr_b16 a[210:211], v12 offset:37888\n\t"
"v_mfma_f32_16x16x32_bf16 a[60:63], a[160:163], v[212:215], a[60:63]\n\t"
"v_mfma_f32_16x16x32_bf16 a[72:75], a[160:163], v[220:223], a[72:75]\n\t"
"s_add_u32 m0, s26, 24576\n\t"
"s_add_u32 s40, s6, 2097152\n\t"
"ds_read_b64_tr_b16 a[212:213], v13 offset:36864\n\t"
"v_mfma_f32_16x16x32_bf16 a[72:75], a[176:179], v[224:227], a[72:75]\n\t"
"buffer_load_dwordx4 v4, s[12:15], s40 offen lds\n\t"
"v_mfma_f32_16x16x32_bf16 a[76:79], a[180:183], v[224:227], a[76:79]\n\t"
"ds_read_b64_tr_b16 a[214:215], v13 offset:37888\n\t"
"v_mfma_f32_16x16x32_bf16 a[76:79], a[164:167], v[220:223], a[76:79]\n\t"
"v_mfma_f32_16x16x32_bf16 a[80:83], a[168:171], v[220:223], a[80:83]\n\t"
"ds_read_b64_tr_b16 a[216:217], v14 offset:36864\n\t"
"v_mfma_f32_16x16x32_bf16 a[80:83], a[184:187], v[224:227], a[80:83]\n\t"
"buffer_load_dwordx4 v5, s[12:15], s40 offen offset:1024 lds\n\t"
"v_mfma_f32_16x16x32_bf16 v[88:91], a[188:191], v[224:227], v[88:91]\n\t"
"ds_read_b64_tr_b16 a[218:219], v14 offset:37888\n\t"
"v_mfma_f32_16x16x32_bf16 v[88:91], a[172:175], v[220:223], v[88:91]\n\t"
"ds_read_b64_tr_b16 a[220:221], v15 offset:36864\n\t"
"v_mfma_f32_16x16x32_bf16 v[92:95], a[172:175], v[44:47], v[92:95]\n\t"
"ds_read_b64_tr_b16 a[222:223], v15 offset:37888\n\t"
"v_mfma_f32_16x16x32_bf16 v[92:95], a[188:191], v[128:131], v[92:95]\n\t"
"buffer_load_dwordx4 v6, s[12:15], s40 offen offset:2048 lds\n\t"
"v_mfma_f32_16x16x32_bf16 a[92:95], a[184:187], v[128:131], a[92:95]\n\t"
"v_mfma_f32_16x16x32_bf16 a[92:95], a[168:171], v[44:47], a[92:95]\n\t"
"v_mfma_f32_16x16x32_bf16 a[88:91], a[164:167], v[44:47], a[88:91]\n\t"
"v_mfma_f32_16x16x32_bf16 a[88:91], a[180:183], v[128:131], a[88:91]\n\t"
"buffer_load_dwordx4 v7, s[12:15], s40 offen offset:3072 lds\n\t"
"v_mfma_f32_16x16x32_bf16 a[84:87], a[176:179], v[128:131], a[84:87]\n\t"
"v_mfma_f32_16x16x32_bf16 a[84:87], a[160:163], v[44:47], a[84:87]\n\t"
"s_waitcnt lgkmcnt(0)\n\t"
"v_mfma_f32_16x16x32_bf16 a[0:3], a[192:195], v[228:231], a[0:3]\n\t"
"s_add_u32 m0, s26, 20480\n\t"
"s_add_u32 s41, s6, 2129920\n\t"
"v_mfma_f32_16x16x32_bf16 a[0:3], a[208:211], v[232:235], a[0:3]\n\t"
"buffer_load_dwordx4 v4, s[12:15], s41 offen lds\n\t"
"v_mfma_f32_16x16x32_bf16 a[4:7], a[212:215], v[232:235], a[4:7]\n\t"
"v_mfma_f32_16x16x32_bf16 a[4:7], a[196:199], v[228:231], a[4:7]\n\t"
"v_mfma_f32_16x16x32_bf16 a[8:11], a[200:203], v[228:231], a[8:11]\n\t"
"v_mfma_f32_16x16x32_bf16 a[8:11], a[216:219], v[232:235], a[8:11]\n\t"
"buffer_load_dwordx4 v5, s[12:15], s41 offen offset:1024 lds\n\t"
"v_mfma_f32_16x16x32_bf16 v[64:67], a[220:223], v[232:235], v[64:67]\n\t"
"v_mfma_f32_16x16x32_bf16 v[64:67], a[204:207], v[228:231], v[64:67]\n\t"
"v_mfma_f32_16x16x32_bf16 v[68:71], a[204:207], v[236:239], v[68:71]\n\t"
"v_mfma_f32_16x16x32_bf16 v[68:71], a[220:223], v[240:243], v[68:71]\n\t"
"buffer_load_dwordx4 v6, s[12:15], s41 offen offset:2048 lds\n\t"
"v_mfma_f32_16x16x32_bf16 a[20:23], a[216:219], v[240:243], a[20:23]\n\t"
"v_mfma_f32_16x16x32_bf16 a[20:23], a[200:203], v[236:239], a[20:23]\n\t"
"v_mov_b32_dpp v152, v108 row_shr:15 row_mask:0xf bank_mask:0x8\n\t"
"v_mfma_f32_16x16x32_bf16 a[16:19], a[196:199], v[236:239], a[16:19]\n\t"
"v_mfma_f32_16x16x32_bf16 a[16:19], a[212:215], v[240:243], a[16:19]\n\t"
"buffer_load_dwordx4 v7, s[12:15], s41 offen offset:3072 lds\n\t"
"v_mov_b32_dpp v153, v109 row_shr:15 row_mask:0xf bank_mask:0x8\n\t"
"v_mfma_f32_16x16x32_bf16 a[12:15], a[208:211], v[240:243], a[12:15]\n\t"
"v_mfma_f32_16x16x32_bf16 a[12:15], a[192:195], v[236:239], a[12:15]\n\t"
"v_mov_b32_dpp v154, v110 row_shr:15 row_mask:0xf bank_mask:0x8\n\t"
"v_mfma_f32_16x16x32_bf16 a[24:27], a[192:195], v[244:247], a[24:27]\n\t"
"s_add_u32 m0, s26, 4096\n\t"
"s_add_u32 s42, s6, 3145728\n\t"
"v_mfma_f32_16x16x32_bf16 a[24:27], a[208:211], v[248:251], a[24:27]\n\t"
"buffer_load_dwordx4 v4, s[12:15], s42 offen lds\n\t"
"v_mfma_f32_16x16x32_bf16 a[28:31], a[212:215], v[248:251], a[28:31]\n\t"
"v_mfma_f32_16x16x32_bf16 a[28:31], a[196:199], v[244:247], a[28:31]\n\t"
"v_mov_b32_dpp v155, v111 row_shr:15 row_mask:0xf bank_mask:0x8\n\t"
"v_mfma_f32_16x16x32_bf16 a[32:35], a[200:203], v[244:247], a[32:35]\n\t"
"v_mfma_f32_16x16x32_bf16 a[32:35], a[216:219], v[248:251], a[32:35]\n\t"
"buffer_load_dwordx4 v5, s[12:15], s42 offen offset:1024 lds\n\t"
"v_mfma_f32_16x16x32_bf16 v[72:75], a[220:223], v[248:251], v[72:75]\n\t"
"v_mfma_f32_16x16x32_bf16 v[72:75], a[204:207], v[244:247], v[72:75]\n\t"
"v_mov_b32_dpp v152, v228 row_shl:1 row_mask:0xf bank_mask:0xf\n\t"
"v_mfma_f32_16x16x32_bf16 v[76:79], a[204:207], v[252:255], v[76:79]\n\t"
"v_mfma_f32_16x16x32_bf16 v[76:79], a[220:223], v[40:43], v[76:79]\n\t"
"buffer_load_dwordx4 v6, s[12:15], s42 offen offset:2048 lds\n\t"
"v_mfma_f32_16x16x32_bf16 a[44:47], a[216:219], v[40:43], a[44:47]\n\t"
"v_mov_b32_dpp v153, v229 row_shl:1 row_mask:0xf bank_mask:0xf\n\t"
"v_mfma_f32_16x16x32_bf16 a[44:47], a[200:203], v[252:255], a[44:47]\n\t"
"v_mov_b32_dpp v154, v230 row_shl:1 row_mask:0xf bank_mask:0xf\n\t"
"v_mfma_f32_16x16x32_bf16 a[40:43], a[196:199], v[252:255], a[40:43]\n\t"
"v_mov_b32_dpp v155, v231 row_shl:1 row_mask:0xf bank_mask:0xf\n\t"
"v_mfma_f32_16x16x32_bf16 a[40:43], a[212:215], v[40:43], a[40:43]\n\t"
"buffer_load_dwordx4 v7, s[12:15], s42 offen offset:3072 lds\n\t"
"v_mfma_f32_16x16x32_bf16 a[36:39], a[208:211], v[40:43], a[36:39]\n\t"
"v_mfma_f32_16x16x32_bf16 a[36:39], a[192:195], v[252:255], a[36:39]\n\t"
"v_mfma_f32_16x16x32_bf16 a[48:51], a[192:195], v[48:51], a[48:51]\n\t"
"s_add_u32 m0, s26, 12288\n\t"
"s_add_u32 s40, s6, 3178496\n\t"
"v_mfma_f32_16x16x32_bf16 a[48:51], a[208:211], v[52:55], a[48:51]\n\t"
"buffer_load_dwordx4 v4, s[12:15], s40 offen lds\n\t"
"v_mfma_f32_16x16x32_bf16 a[52:55], a[212:215], v[52:55], a[52:55]\n\t"
"s_waitcnt vmcnt(19)\n\t"
"ds_read_b64_tr_b16 a[224:225], v12 offset:28672\n\t"
"v_mfma_f32_16x16x32_bf16 a[52:55], a[196:199], v[48:51], a[52:55]\n\t"
"ds_read_b64_tr_b16 a[226:227], v12 offset:29696\n\t"
"v_mfma_f32_16x16x32_bf16 a[56:59], a[200:203], v[48:51], a[56:59]\n\t"
"ds_read_b64_tr_b16 a[228:229], v13 offset:28672\n\t"
"v_mfma_f32_16x16x32_bf16 a[56:59], a[216:219], v[52:55], a[56:59]\n\t"
"buffer_load_dwordx4 v5, s[12:15], s40 offen offset:1024 lds\n\t"
"v_mfma_f32_16x16x32_bf16 v[80:83], a[220:223], v[52:55], v[80:83]\n\t"
"ds_read_b64_tr_b16 a[230:231], v13 offset:29696\n\t"
"v_mfma_f32_16x16x32_bf16 v[80:83], a[204:207], v[48:51], v[80:83]\n\t"
"ds_read_b64_tr_b16 a[232:233], v14 offset:28672\n\t"
"v_mfma_f32_16x16x32_bf16 v[84:87], a[204:207], v[56:59], v[84:87]\n\t"
"ds_read_b64_tr_b16 a[234:235], v14 offset:29696\n\t"
"v_mfma_f32_16x16x32_bf16 v[84:87], a[220:223], v[60:63], v[84:87]\n\t"
"buffer_load_dwordx4 v6, s[12:15], s40 offen offset:2048 lds\n\t"
"v_mfma_f32_16x16x32_bf16 a[68:71], a[216:219], v[60:63], a[68:71]\n\t"
"ds_read_b64_tr_b16 a[236:237], v15 offset:28672\n\t"
"v_mfma_f32_16x16x32_bf16 a[68:71], a[200:203], v[56:59], a[68:71]\n\t"
"ds_read_b64_tr_b16 a[238:239], v15 offset:29696\n\t"
"v_mov_b32_dpp v156, v112 row_shr:15 row_mask:0xf bank_mask:0x8\n\t"
"v_mfma_f32_16x16x32_bf16 a[64:67], a[196:199], v[56:59], a[64:67]\n\t"
"s_waitcnt vmcnt(15)\n\t"
"ds_read_b64_tr_b16 a[240:241], v12 offset:0\n\t"
"v_mfma_f32_16x16x32_bf16 a[64:67], a[212:215], v[60:63], a[64:67]\n\t"
"buffer_load_dwordx4 v7, s[12:15], s40 offen offset:3072 lds\n\t"
"v_mov_b32_dpp v157, v113 row_shr:15 row_mask:0xf bank_mask:0x8\n\t"
"v_mfma_f32_16x16x32_bf16 a[60:63], a[208:211], v[60:63], a[60:63]\n\t"
"ds_read_b64_tr_b16 a[242:243], v12 offset:1024\n\t"
"v_mfma_f32_16x16x32_bf16 a[60:63], a[192:195], v[56:59], a[60:63]\n\t"
"v_mov_b32_dpp v158, v114 row_shr:15 row_mask:0xf bank_mask:0x8\n\t"
"v_mfma_f32_16x16x32_bf16 a[72:75], a[192:195], v[116:119], a[72:75]\n\t"
"s_add_u32 m0, s26, 8192\n\t"
"s_add_u32 s41, s6, 4194304\n\t"
"ds_read_b64_tr_b16 a[244:245], v13 offset:0\n\t"
"v_mfma_f32_16x16x32_bf16 a[72:75], a[208:211], v[120:123], a[72:75]\n\t"
"buffer_load_dwordx4 v4, s[12:15], s41 offen lds\n\t"
"v_mfma_f32_16x16x32_bf16 a[76:79], a[212:215], v[120:123], a[76:79]\n\t"
"ds_read_b64_tr_b16 a[246:247], v13 offset:1024\n\t"
"v_mfma_f32_16x16x32_bf16 a[76:79], a[196:199], v[116:119], a[76:79]\n\t"
"v_mov_b32_dpp v159, v115 row_shr:15 row_mask:0xf bank_mask:0x8\n\t"
"v_mfma_f32_16x16x32_bf16 a[80:83], a[200:203], v[116:119], a[80:83]\n\t"
"ds_read_b64_tr_b16 a[248:249], v14 offset:0\n\t"
"v_mfma_f32_16x16x32_bf16 a[80:83], a[216:219], v[120:123], a[80:83]\n\t"
"buffer_load_dwordx4 v5, s[12:15], s41 offen offset:1024 lds\n\t"
"v_mfma_f32_16x16x32_bf16 v[88:91], a[220:223], v[120:123], v[88:91]\n\t"
"ds_read_b64_tr_b16 a[250:251], v14 offset:1024\n\t"
"v_mfma_f32_16x16x32_bf16 v[88:91], a[204:207], v[116:119], v[88:91]\n\t"
"ds_read_b64_tr_b16 a[252:253], v15 offset:0\n\t"
"v_mov_b32_dpp v156, v232 row_shl:1 row_mask:0xf bank_mask:0xf\n\t"
"v_mfma_f32_16x16x32_bf16 v[92:95], a[204:207], v[124:127], v[92:95]\n\t"
"ds_read_b64_tr_b16 a[254:255], v15 offset:1024\n\t"
"v_mfma_f32_16x16x32_bf16 v[92:95], a[220:223], v[148:151], v[92:95]\n\t"
"buffer_load_dwordx4 v6, s[12:15], s41 offen offset:2048 lds\n\t"
"v_mfma_f32_16x16x32_bf16 a[92:95], a[216:219], v[148:151], a[92:95]\n\t"
"v_mov_b32_dpp v157, v233 row_shl:1 row_mask:0xf bank_mask:0xf\n\t"
"v_mfma_f32_16x16x32_bf16 a[92:95], a[200:203], v[124:127], a[92:95]\n\t"
"v_mov_b32_dpp v158, v234 row_shl:1 row_mask:0xf bank_mask:0xf\n\t"
"v_mfma_f32_16x16x32_bf16 a[88:91], a[196:199], v[124:127], a[88:91]\n\t"
"v_mov_b32_dpp v159, v235 row_shl:1 row_mask:0xf bank_mask:0xf\n\t"
"v_mfma_f32_16x16x32_bf16 a[88:91], a[212:215], v[148:151], a[88:91]\n\t"
"buffer_load_dwordx4 v7, s[12:15], s41 offen offset:3072 lds\n\t"
"v_mfma_f32_16x16x32_bf16 a[84:87], a[208:211], v[148:151], a[84:87]\n\t"
"v_mfma_f32_16x16x32_bf16 a[84:87], a[192:195], v[124:127], a[84:87]\n\t"
"s_xor_b32 s43, s33, 6356992\n\t"
"buffer_load_dword v24, v96, s[12:15], s43 offen\n\t"
"s_xor_b32 s43, s33, 524288\n\t"
"buffer_load_dword v24, v97, s[12:15], s43 offen\n\t"
"s_waitcnt lgkmcnt(0)\n\t"
"v_mfma_f32_16x16x32_bf16 a[0:3], a[224:227], v[236:239], a[0:3]\n\t"
"v_mfma_f32_16x16x32_bf16 a[0:3], a[240:243], v[240:243], a[0:3]\n\t"
"v_mfma_f32_16x16x32_bf16 a[4:7], a[244:247], v[240:243], a[4:7]\n\t"
"v_mfma_f32_16x16x32_bf16 a[4:7], a[228:231], v[236:239], a[4:7]\n\t"
"v_mfma_f32_16x16x32_bf16 a[8:11], a[232:235], v[236:239], a[8:11]\n\t"
"v_mfma_f32_16x16x32_bf16 a[8:11], a[248:251], v[240:243], a[8:11]\n\t"
"v_mfma_f32_16x16x32_bf16 v[64:67], a[252:255], v[240:243], v[64:67]\n\t"
"v_mfma_f32_16x16x32_bf16 v[64:67], a[236:239], v[236:239], v[64:67]\n\t"
"v_mfma_f32_16x16x32_bf16 v[68:71], a[236:239], v[244:247], v[68:71]\n\t"
"v_mfma_f32_16x16x32_bf16 v[68:71], a[252:255], v[248:251], v[68:71]\n\t"
"v_mfma_f32_16x16x32_bf16 a[20:23], a[248:251], v[248:251], a[20:23]\n\t"
"v_mfma_f32_16x16x32_bf16 a[20:23], a[232:235], v[244:247], a[20:23]\n\t"
"v_mov_b32_dpp v160, v108 row_shr:14 row_mask:0xf bank_mask:0x8\n\t"
"v_mfma_f32_16x16x32_bf16 a[16:19], a[228:231], v[244:247], a[16:19]\n\t"
"v_mfma_f32_16x16x32_bf16 a[16:19], a[244:247], v[248:251], a[16:19]\n\t"
"v_mov_b32_dpp v161, v109 row_shr:14 row_mask:0xf bank_mask:0x8\n\t"
"v_mfma_f32_16x16x32_bf16 a[12:15], a[240:243], v[248:251], a[12:15]\n\t"
"v_mfma_f32_16x16x32_bf16 a[12:15], a[224:227], v[244:247], a[12:15]\n\t"
"v_mov_b32_dpp v162, v110 row_shr:14 row_mask:0xf bank_mask:0x8\n\t"
"v_mfma_f32_16x16x32_bf16 a[24:27], a[224:227], v[252:255], a[24:27]\n\t"
"v_mfma_f32_16x16x32_bf16 a[24:27], a[240:243], v[40:43], a[24:27]\n\t"
"v_mfma_f32_16x16x32_bf16 a[28:31], a[244:247], v[40:43], a[28:31]\n\t"
"v_mfma_f32_16x16x32_bf16 a[28:31], a[228:231], v[252:255], a[28:31]\n\t"
"v_mov_b32_dpp v163, v111 row_shr:14 row_mask:0xf bank_mask:0x8\n\t"
"v_mfma_f32_16x16x32_bf16 a[32:35], a[232:235], v[252:255], a[32:35]\n\t"
"v_mfma_f32_16x16x32_bf16 a[32:35], a[248:251], v[40:43], a[32:35]\n\t"
"v_mfma_f32_16x16x32_bf16 v[72:75], a[252:255], v[40:43], v[72:75]\n\t"
"v_mfma_f32_16x16x32_bf16 v[72:75], a[236:239], v[252:255], v[72:75]\n\t"
"v_mov_b32_dpp v160, v236 row_shl:1 row_mask:0xf bank_mask:0xf\n\t"
"v_mfma_f32_16x16x32_bf16 v[76:79], a[236:239], v[48:51], v[76:79]\n\t"
"v_mfma_f32_16x16x32_bf16 v[76:79], a[252:255], v[52:55], v[76:79]\n\t"
"v_mfma_f32_16x16x32_bf16 a[44:47], a[248:251], v[52:55], a[44:47]\n\t"
"v_mov_b32_dpp v161, v237 row_shl:1 row_mask:0xf bank_mask:0xf\n\t"
"v_mfma_f32_16x16x32_bf16 a[44:47], a[232:235], v[48:51], a[44:47]\n\t"
"v_mov_b32_dpp v162, v238 row_shl:1 row_mask:0xf bank_mask:0xf\n\t"
"v_mfma_f32_16x16x32_bf16 a[40:43], a[228:231], v[48:51], a[40:43]\n\t"
"v_mov_b32_dpp v163, v239 row_shl:1 row_mask:0xf bank_mask:0xf\n\t"
"v_mfma_f32_16x16x32_bf16 a[40:43], a[244:247], v[52:55], a[40:43]\n\t"
"v_mfma_f32_16x16x32_bf16 a[36:39], a[240:243], v[52:55], a[36:39]\n\t"
"v_mfma_f32_16x16x32_bf16 a[36:39], a[224:227], v[48:51], a[36:39]\n\t"
"v_mfma_f32_16x16x32_bf16 a[48:51], a[224:227], v[56:59], a[48:51]\n\t"
"s_add_u32 m0, s26, 16384\n\t"
"s_add_u32 s42, s6, 4227072\n\t"
"v_mfma_f32_16x16x32_bf16 a[48:51], a[240:243], v[60:63], a[48:51]\n\t"
"buffer_load_dwordx4 v4, s[12:15], s42 offen lds\n\t"
"v_mfma_f32_16x16x32_bf16 a[52:55], a[244:247], v[60:63], a[52:55]\n\t"
"s_waitcnt vmcnt(19)\n\t"
"ds_read_b64_tr_b16 a[96:97], v12 offset:24576\n\t"
"v_mfma_f32_16x16x32_bf16 a[52:55], a[228:231], v[56:59], a[52:55]\n\t"
"ds_read_b64_tr_b16 a[98:99], v12 offset:25600\n\t"
"v_mfma_f32_16x16x32_bf16 a[56:59], a[232:235], v[56:59], a[56:59]\n\t"
"ds_read_b64_tr_b16 a[100:101], v13 offset:24576\n\t"
"v_mfma_f32_16x16x32_bf16 a[56:59], a[248:251], v[60:63], a[56:59]\n\t"
"v_mfma_f32_16x16x32_bf16 v[80:83], a[252:255], v[60:63], v[80:83]\n\t"
"ds_read_b64_tr_b16 a[102:103], v13 offset:25600\n\t"
"v_mfma_f32_16x16x32_bf16 v[80:83], a[236:239], v[56:59], v[80:83]\n\t"
"ds_read_b64_tr_b16 a[104:105], v14 offset:24576\n\t"
"v_mfma_f32_16x16x32_bf16 v[84:87], a[236:239], v[116:119], v[84:87]\n\t"
"ds_read_b64_tr_b16 a[106:107], v14 offset:25600\n\t"
"v_mfma_f32_16x16x32_bf16 v[84:87], a[252:255], v[120:123], v[84:87]\n\t"
"buffer_load_dwordx4 v5, s[12:15], s42 offen offset:1024 lds\n\t"
"v_mfma_f32_16x16x32_bf16 a[68:71], a[248:251], v[120:123], a[68:71]\n\t"
"ds_read_b64_tr_b16 a[108:109], v15 offset:24576\n\t"
"v_mfma_f32_16x16x32_bf16 a[68:71], a[232:235], v[116:119], a[68:71]\n\t"
"ds_read_b64_tr_b16 a[110:111], v15 offset:25600\n\t"
"v_mov_b32_dpp v132, v112 row_shr:14 row_mask:0xf bank_mask:0x8\n\t"
"v_mfma_f32_16x16x32_bf16 a[64:67], a[228:231], v[116:119], a[64:67]\n\t"
"s_waitcnt vmcnt(16)\n\t"
"ds_read_b64_tr_b16 a[112:113], v12 offset:20480\n\t"
"v_mfma_f32_16x16x32_bf16 a[64:67], a[244:247], v[120:123], a[64:67]\n\t"
"v_mov_b32_dpp v133, v113 row_shr:14 row_mask:0xf bank_mask:0x8\n\t"
"v_mfma_f32_16x16x32_bf16 a[60:63], a[240:243], v[120:123], a[60:63]\n\t"
"ds_read_b64_tr_b16 a[114:115], v12 offset:21504\n\t"
"v_mfma_f32_16x16x32_bf16 a[60:63], a[224:227], v[116:119], a[60:63]\n\t"
"v_mov_b32_dpp v134, v114 row_shr:14 row_mask:0xf bank_mask:0x8\n\t"
"v_mfma_f32_16x16x32_bf16 a[72:75], a[224:227], v[124:127], a[72:75]\n\t"
"ds_read_b64_tr_b16 a[116:117], v13 offset:20480\n\t"
"v_mfma_f32_16x16x32_bf16 a[72:75], a[240:243], v[148:151], a[72:75]\n\t"
"buffer_load_dwordx4 v6, s[12:15], s42 offen offset:2048 lds\n\t"
"v_mfma_f32_16x16x32_bf16 a[76:79], a[244:247], v[148:151], a[76:79]\n\t"
"ds_read_b64_tr_b16 a[118:119], v13 offset:21504\n\t"
"v_mfma_f32_16x16x32_bf16 a[76:79], a[228:231], v[124:127], a[76:79]\n\t"
"v_mov_b32_dpp v135, v115 row_shr:14 row_mask:0xf bank_mask:0x8\n\t"
"v_mfma_f32_16x16x32_bf16 a[80:83], a[232:235], v[124:127], a[80:83]\n\t"
"ds_read_b64_tr_b16 a[120:121], v14 offset:20480\n\t"
"v_mfma_f32_16x16x32_bf16 a[80:83], a[248:251], v[148:151], a[80:83]\n\t"
"v_mfma_f32_16x16x32_bf16 v[88:91], a[252:255], v[148:151], v[88:91]\n\t"
"ds_read_b64_tr_b16 a[122:123], v14 offset:21504\n\t"
"v_mfma_f32_16x16x32_bf16 v[88:91], a[236:239], v[124:127], v[88:91]\n\t"
"ds_read_b64_tr_b16 a[124:125], v15 offset:20480\n\t"
"v_mov_b32_dpp v132, v240 row_shl:1 row_mask:0xf bank_mask:0xf\n\t"
"v_mfma_f32_16x16x32_bf16 v[92:95], a[236:239], v[152:155], v[92:95]\n\t"
"ds_read_b64_tr_b16 a[126:127], v15 offset:21504\n\t"
"v_mfma_f32_16x16x32_bf16 v[92:95], a[252:255], v[156:159], v[92:95]\n\t"
"buffer_load_dwordx4 v7, s[12:15], s42 offen offset:3072 lds\n\t"
"v_mfma_f32_16x16x32_bf16 a[92:95], a[248:251], v[156:159], a[92:95]\n\t"
"v_mov_b32_dpp v133, v241 row_shl:1 row_mask:0xf bank_mask:0xf\n\t"
"v_mfma_f32_16x16x32_bf16 a[92:95], a[232:235], v[152:155], a[92:95]\n\t"
"v_mov_b32_dpp v134, v242 row_shl:1 row_mask:0xf bank_mask:0xf\n\t"
"v_mfma_f32_16x16x32_bf16 a[88:91], a[228:231], v[152:155], a[88:91]\n\t"
"v_mov_b32_dpp v135, v243 row_shl:1 row_mask:0xf bank_mask:0xf\n\t"
"v_mfma_f32_16x16x32_bf16 a[88:91], a[244:247], v[156:159], a[88:91]\n\t"
"v_mfma_f32_16x16x32_bf16 a[84:87], a[240:243], v[156:159], a[84:87]\n\t"
"v_mfma_f32_16x16x32_bf16 a[84:87], a[224:227], v[152:155], a[84:87]\n\t"
"s_waitcnt lgkmcnt(0)\n\t"
"v_mfma_f32_16x16x32_bf16 a[0:3], a[96:99], v[244:247], a[0:3]\n\t"
"s_add_u32 m0, s26, 32768\n\t"
"s_add_u32 s40, s6, 5242880\n\t"
"v_mfma_f32_16x16x32_bf16 a[0:3], a[112:115], v[248:251], a[0:3]\n\t"
"buffer_load_dwordx4 v4, s[12:15], s40 offen lds\n\t"
"v_mfma_f32_16x16x32_bf16 a[4:7], a[116:119], v[248:251], a[4:7]\n\t"
"v_mfma_f32_16x16x32_bf16 a[4:7], a[100:103], v[244:247], a[4:7]\n\t"
"v_mfma_f32_16x16x32_bf16 a[8:11], a[104:107], v[244:247], a[8:11]\n\t"
"v_mfma_f32_16x16x32_bf16 a[8:11], a[120:123], v[248:251], a[8:11]\n\t"
"buffer_load_dwordx4 v5, s[12:15], s40 offen offset:1024 lds\n\t"
"v_mfma_f32_16x16x32_bf16 v[64:67], a[124:127], v[248:251], v[64:67]\n\t"
"v_mfma_f32_16x16x32_bf16 v[64:67], a[108:111], v[244:247], v[64:67]\n\t"
"v_mfma_f32_16x16x32_bf16 v[68:71], a[108:111], v[252:255], v[68:71]\n\t"
"v_mfma_f32_16x16x32_bf16 v[68:71], a[124:127], v[40:43], v[68:71]\n\t"
"buffer_load_dwordx4 v6, s[12:15], s40 offen offset:2048 lds\n\t"
"v_mfma_f32_16x16x32_bf16 a[20:23], a[120:123], v[40:43], a[20:23]\n\t"
"v_mfma_f32_16x16x32_bf16 a[20:23], a[104:107], v[252:255], a[20:23]\n\t"
"v_mov_b32_dpp v136, v108 row_shr:13 row_mask:0xf bank_mask:0x8\n\t"
"v_mfma_f32_16x16x32_bf16 a[16:19], a[100:103], v[252:255], a[16:19]\n\t"
"v_mfma_f32_16x16x32_bf16 a[16:19], a[116:119], v[40:43], a[16:19]\n\t"
"buffer_load_dwordx4 v7, s[12:15], s40 offen offset:3072 lds\n\t"
"v_mov_b32_dpp v137, v109 row_shr:13 row_mask:0xf bank_mask:0x8\n\t"
"v_mfma_f32_16x16x32_bf16 a[12:15], a[112:115], v[40:43], a[12:15]\n\t"
"v_mfma_f32_16x16x32_bf16 a[12:15], a[96:99], v[252:255], a[12:15]\n\t"
"v_mov_b32_dpp v138, v110 row_shr:13 row_mask:0xf bank_mask:0x8\n\t"
"v_mfma_f32_16x16x32_bf16 a[24:27], a[96:99], v[48:51], a[24:27]\n\t"
"s_add_u32 m0, s26, 36864\n\t"
"s_add_u32 s41, s6, 5275648\n\t"
"v_mfma_f32_16x16x32_bf16 a[24:27], a[112:115], v[52:55], a[24:27]\n\t"
"buffer_load_dwordx4 v4, s[12:15], s41 offen lds\n\t"
"v_mfma_f32_16x16x32_bf16 a[28:31], a[116:119], v[52:55], a[28:31]\n\t"
"v_mfma_f32_16x16x32_bf16 a[28:31], a[100:103], v[48:51], a[28:31]\n\t"
"v_mov_b32_dpp v139, v111 row_shr:13 row_mask:0xf bank_mask:0x8\n\t"
"v_mfma_f32_16x16x32_bf16 a[32:35], a[104:107], v[48:51], a[32:35]\n\t"
"v_mfma_f32_16x16x32_bf16 a[32:35], a[120:123], v[52:55], a[32:35]\n\t"
"buffer_load_dwordx4 v5, s[12:15], s41 offen offset:1024 lds\n\t"
"v_mfma_f32_16x16x32_bf16 v[72:75], a[124:127], v[52:55], v[72:75]\n\t"
"v_mfma_f32_16x16x32_bf16 v[72:75], a[108:111], v[48:51], v[72:75]\n\t"
"v_mov_b32_dpp v136, v244 row_shl:1 row_mask:0xf bank_mask:0xf\n\t"
"v_mfma_f32_16x16x32_bf16 v[76:79], a[108:111], v[56:59], v[76:79]\n\t"
"v_mfma_f32_16x16x32_bf16 v[76:79], a[124:127], v[60:63], v[76:79]\n\t"
"buffer_load_dwordx4 v6, s[12:15], s41 offen offset:2048 lds\n\t"
"v_mfma_f32_16x16x32_bf16 a[44:47], a[120:123], v[60:63], a[44:47]\n\t"
"v_mov_b32_dpp v137, v245 row_shl:1 row_mask:0xf bank_mask:0xf\n\t"
"v_mfma_f32_16x16x32_bf16 a[44:47], a[104:107], v[56:59], a[44:47]\n\t"
"v_mov_b32_dpp v138, v246 row_shl:1 row_mask:0xf bank_mask:0xf\n\t"
"v_mfma_f32_16x16x32_bf16 a[40:43], a[100:103], v[56:59], a[40:43]\n\t"
"v_mov_b32_dpp v139, v247 row_shl:1 row_mask:0xf bank_mask:0xf\n\t"
"v_mfma_f32_16x16x32_bf16 a[40:43], a[116:119], v[60:63], a[40:43]\n\t"
"buffer_load_dwordx4 v7, s[12:15], s41 offen offset:3072 lds\n\t"
"v_mfma_f32_16x16x32_bf16 a[36:39], a[112:115], v[60:63], a[36:39]\n\t"
"v_mfma_f32_16x16x32_bf16 a[36:39], a[96:99], v[56:59], a[36:39]\n\t"
"v_mfma_f32_16x16x32_bf16 a[48:51], a[96:99], v[116:119], a[48:51]\n\t"
"s_add_u32 m0, s26, 28672\n\t"
"s_add_u32 s42, s6, 6291456\n\t"
"v_mfma_f32_16x16x32_bf16 a[48:51], a[112:115], v[120:123], a[48:51]\n\t"
"buffer_load_dwordx4 v4, s[12:15], s42 offen lds\n\t"
"v_mfma_f32_16x16x32_bf16 a[52:55], a[116:119], v[120:123], a[52:55]\n\t"
"s_waitcnt vmcnt(23)\n\t"
"ds_read_b64_tr_b16 a[128:129], v12 offset:4096\n\t"
"v_mfma_f32_16x16x32_bf16 a[52:55], a[100:103], v[116:119], a[52:55]\n\t"
"ds_read_b64_tr_b16 a[130:131], v12 offset:5120\n\t"
"v_mfma_f32_16x16x32_bf16 a[56:59], a[104:107], v[116:119], a[56:59]\n\t"
"ds_read_b64_tr_b16 a[132:133], v13 offset:4096\n\t"
"v_mfma_f32_16x16x32_bf16 a[56:59], a[120:123], v[120:123], a[56:59]\n\t"
"v_mfma_f32_16x16x32_bf16 v[80:83], a[124:127], v[120:123], v[80:83]\n\t"
"ds_read_b64_tr_b16 a[134:135], v13 offset:5120\n\t"
"v_mfma_f32_16x16x32_bf16 v[80:83], a[108:111], v[116:119], v[80:83]\n\t"
"ds_read_b64_tr_b16 a[136:137], v14 offset:4096\n\t"
"v_mfma_f32_16x16x32_bf16 v[84:87], a[108:111], v[124:127], v[84:87]\n\t"
"ds_read_b64_tr_b16 a[138:139], v14 offset:5120\n\t"
"v_mfma_f32_16x16x32_bf16 v[84:87], a[124:127], v[148:151], v[84:87]\n\t"
"buffer_load_dwordx4 v5, s[12:15], s42 offen offset:1024 lds\n\t"
"v_mfma_f32_16x16x32_bf16 a[68:71], a[120:123], v[148:151], a[68:71]\n\t"
"ds_read_b64_tr_b16 a[140:141], v15 offset:4096\n\t"
"v_mfma_f32_16x16x32_bf16 a[68:71], a[104:107], v[124:127], a[68:71]\n\t"
"ds_read_b64_tr_b16 a[142:143], v15 offset:5120\n\t"
"v_mov_b32_dpp v140, v112 row_shr:13 row_mask:0xf bank_mask:0x8\n\t"
"v_mfma_f32_16x16x32_bf16 a[64:67], a[100:103], v[124:127], a[64:67]\n\t"
"s_waitcnt vmcnt(20)\n\t"
"ds_read_b64_tr_b16 a[144:145], v12 offset:12288\n\t"
"v_mfma_f32_16x16x32_bf16 a[64:67], a[116:119], v[148:151], a[64:67]\n\t"
"v_mov_b32_dpp v141, v113 row_shr:13 row_mask:0xf bank_mask:0x8\n\t"
"v_mfma_f32_16x16x32_bf16 a[60:63], a[112:115], v[148:151], a[60:63]\n\t"
"ds_read_b64_tr_b16 a[146:147], v12 offset:13312\n\t"
"v_mfma_f32_16x16x32_bf16 a[60:63], a[96:99], v[124:127], a[60:63]\n\t"
"v_mov_b32_dpp v142, v114 row_shr:13 row_mask:0xf bank_mask:0x8\n\t"
"v_mfma_f32_16x16x32_bf16 a[72:75], a[96:99], v[152:155], a[72:75]\n\t"
"ds_read_b64_tr_b16 a[148:149], v13 offset:12288\n\t"
"v_mfma_f32_16x16x32_bf16 a[72:75], a[112:115], v[156:159], a[72:75]\n\t"
"buffer_load_dwordx4 v6, s[12:15], s42 offen offset:2048 lds\n\t"
"v_mfma_f32_16x16x32_bf16 a[76:79], a[116:119], v[156:159], a[76:79]\n\t"
"ds_read_b64_tr_b16 a[150:151], v13 offset:13312\n\t"
"v_mfma_f32_16x16x32_bf16 a[76:79], a[100:103], v[152:155], a[76:79]\n\t"
"v_mov_b32_dpp v143, v115 row_shr:13 row_mask:0xf bank_mask:0x8\n\t"
"v_mfma_f32_16x16x32_bf16 a[80:83], a[104:107], v[152:155], a[80:83]\n\t"
"ds_read_b64_tr_b16 a[152:153], v14 offset:12288\n\t"
"v_mfma_f32_16x16x32_bf16 a[80:83], a[120:123], v[156:159], a[80:83]\n\t"
"v_mfma_f32_16x16x32_bf16 v[88:91], a[124:127], v[156:159], v[88:91]\n\t"
"ds_read_b64_tr_b16 a[154:155], v14 offset:13312\n\t"
"v_mfma_f32_16x16x32_bf16 v[88:91], a[108:111], v[152:155], v[88:91]\n\t"
"ds_read_b64_tr_b16 a[156:157], v15 offset:12288\n\t"
"v_mov_b32_dpp v140, v248 row_shl:1 row_mask:0xf bank_mask:0xf\n\t"
"v_mfma_f32_16x16x32_bf16 v[92:95], a[108:111], v[160:163], v[92:95]\n\t"
"ds_read_b64_tr_b16 a[158:159], v15 offset:13312\n\t"
"v_mfma_f32_16x16x32_bf16 v[92:95], a[124:127], v[132:135], v[92:95]\n\t"
"buffer_load_dwordx4 v7, s[12:15], s42 offen offset:3072 lds\n\t"
"v_mfma_f32_16x16x32_bf16 a[92:95], a[120:123], v[132:135], a[92:95]\n\t"
"v_mov_b32_dpp v141, v249 row_shl:1 row_mask:0xf bank_mask:0xf\n\t"
"v_mfma_f32_16x16x32_bf16 a[92:95], a[104:107], v[160:163], a[92:95]\n\t"
"v_mov_b32_dpp v142, v250 row_shl:1 row_mask:0xf bank_mask:0xf\n\t"
"v_mfma_f32_16x16x32_bf16 a[88:91], a[100:103], v[160:163], a[88:91]\n\t"
"v_mov_b32_dpp v143, v251 row_shl:1 row_mask:0xf bank_mask:0xf\n\t"
"v_mfma_f32_16x16x32_bf16 a[88:91], a[116:119], v[132:135], a[88:91]\n\t"
"v_mfma_f32_16x16x32_bf16 a[84:87], a[112:115], v[132:135], a[84:87]\n\t"
"v_mfma_f32_16x16x32_bf16 a[84:87], a[96:99], v[160:163], a[84:87]\n\t"
"s_waitcnt lgkmcnt(0)\n\t"
"v_mfma_f32_16x16x32_bf16 a[0:3], a[128:131], v[252:255], a[0:3]\n\t"
"s_add_u32 m0, s26, 0\n\t"
"s_add_u32 s40, s6, 6324224\n\t"
"v_mfma_f32_16x16x32_bf16 a[0:3], a[144:147], v[40:43], a[0:3]\n\t"
"buffer_load_dwordx4 v4, s[12:15], s40 offen lds\n\t"
"v_mfma_f32_16x16x32_bf16 a[4:7], a[148:151], v[40:43], a[4:7]\n\t"
"v_mfma_f32_16x16x32_bf16 a[4:7], a[132:135], v[252:255], a[4:7]\n\t"
"v_mfma_f32_16x16x32_bf16 a[8:11], a[136:139], v[252:255], a[8:11]\n\t"
"v_mfma_f32_16x16x32_bf16 a[8:11], a[152:155], v[40:43], a[8:11]\n\t"
"v_mfma_f32_16x16x32_bf16 v[64:67], a[156:159], v[40:43], v[64:67]\n\t"
"v_mfma_f32_16x16x32_bf16 v[64:67], a[140:143], v[252:255], v[64:67]\n\t"
"v_mfma_f32_16x16x32_bf16 v[68:71], a[140:143], v[48:51], v[68:71]\n\t"
"v_mfma_f32_16x16x32_bf16 v[68:71], a[156:159], v[52:55], v[68:71]\n\t"
"buffer_load_dwordx4 v5, s[12:15], s40 offen offset:1024 lds\n\t"
"v_mfma_f32_16x16x32_bf16 a[20:23], a[152:155], v[52:55], a[20:23]\n\t"
"v_mfma_f32_16x16x32_bf16 a[20:23], a[136:139], v[48:51], a[20:23]\n\t"
"v_mov_b32_dpp v144, v108 row_shr:12 row_mask:0xf bank_mask:0x8\n\t"
"v_mfma_f32_16x16x32_bf16 a[16:19], a[132:135], v[48:51], a[16:19]\n\t"
"v_mfma_f32_16x16x32_bf16 a[16:19], a[148:151], v[52:55], a[16:19]\n\t"
"v_mov_b32_dpp v145, v109 row_shr:12 row_mask:0xf bank_mask:0x8\n\t"
"v_mfma_f32_16x16x32_bf16 a[12:15], a[144:147], v[52:55], a[12:15]\n\t"
"v_mfma_f32_16x16x32_bf16 a[12:15], a[128:131], v[48:51], a[12:15]\n\t"
"v_mov_b32_dpp v146, v110 row_shr:12 row_mask:0xf bank_mask:0x8\n\t"
"v_mfma_f32_16x16x32_bf16 a[24:27], a[128:131], v[56:59], a[24:27]\n\t"
"v_mfma_f32_16x16x32_bf16 a[24:27], a[144:147], v[60:63], a[24:27]\n\t"
"buffer_load_dwordx4 v6, s[12:15], s40 offen offset:2048 lds\n\t"
"v_mfma_f32_16x16x32_bf16 a[28:31], a[148:151], v[60:63], a[28:31]\n\t"
"v_mfma_f32_16x16x32_bf16 a[28:31], a[132:135], v[56:59], a[28:31]\n\t"
"v_mov_b32_dpp v147, v111 row_shr:12 row_mask:0xf bank_mask:0x8\n\t"
"v_mfma_f32_16x16x32_bf16 a[32:35], a[136:139], v[56:59], a[32:35]\n\t"
"v_mfma_f32_16x16x32_bf16 a[32:35], a[152:155], v[60:63], a[32:35]\n\t"
"v_mfma_f32_16x16x32_bf16 v[72:75], a[156:159], v[60:63], v[72:75]\n\t"
"v_mfma_f32_16x16x32_bf16 v[72:75], a[140:143], v[56:59], v[72:75]\n\t"
"v_mov_b32_dpp v144, v252 row_shl:1 row_mask:0xf bank_mask:0xf\n\t"
"v_mfma_f32_16x16x32_bf16 v[76:79], a[140:143], v[116:119], v[76:79]\n\t"
"v_mfma_f32_16x16x32_bf16 v[76:79], a[156:159], v[120:123], v[76:79]\n\t"
"buffer_load_dwordx4 v7, s[12:15], s40 offen offset:3072 lds\n\t"
"v_mfma_f32_16x16x32_bf16 a[44:47], a[152:155], v[120:123], a[44:47]\n\t"
"v_mov_b32_dpp v145, v253 row_shl:1 row_mask:0xf bank_mask:0xf\n\t"
"v_mfma_f32_16x16x32_bf16 a[44:47], a[136:139], v[116:119], a[44:47]\n\t"
"v_mov_b32_dpp v146, v254 row_shl:1 row_mask:0xf bank_mask:0xf\n\t"
"v_mfma_f32_16x16x32_bf16 a[40:43], a[132:135], v[116:119], a[40:43]\n\t"
"v_mov_b32_dpp v147, v255 row_shl:1 row_mask:0xf bank_mask:0xf\n\t"
"v_mfma_f32_16x16x32_bf16 a[40:43], a[148:151], v[120:123], a[40:43]\n\t"
"v_mfma_f32_16x16x32_bf16 a[36:39], a[144:147], v[120:123], a[36:39]\n\t"
"v_mfma_f32_16x16x32_bf16 a[36:39], a[128:131], v[116:119], a[36:39]\n\t"
"s_xor_b32 s43, s27, 1024\n\t"
"s_mov_b32 exec_hi, 3\n\t"
"buffer_load_dword v24, v23, s[8:11], s43 offen\n\t"
"s_mov_b32 exec_hi, -1\n\t"
"buffer_load_dword v26, off, s[16:19], 0 sc1\n\t"
"global_load_short_d16_hi v27, v3, s[30:31]\n\t"
"global_load_short_d16_hi v28, v3, s[4:5]\n\t"
"buffer_load_dword v24, v29, s[16:19], 0 offen sc1\n\t"
"v_mfma_f32_16x16x32_bf16 a[48:51], a[128:131], v[124:127], a[48:51]\n\t"
"s_add_u32 m0, s26, 24576\n\t"
"s_add_u32 s41, s6, 7340032\n\t"
"v_mfma_f32_16x16x32_bf16 a[48:51], a[144:147], v[148:151], a[48:51]\n\t"
"buffer_load_dwordx4 v4, s[12:15], s41 offen lds\n\t"
"v_mfma_f32_16x16x32_bf16 a[52:55], a[148:151], v[148:151], a[52:55]\n\t"
"s_waitcnt vmcnt(28)\n\t"
"ds_read_b64_tr_b16 a[160:161], v12 offset:8192\n\t"
"v_mfma_f32_16x16x32_bf16 a[52:55], a[132:135], v[124:127], a[52:55]\n\t"
"ds_read_b64_tr_b16 a[162:163], v12 offset:9216\n\t"
"v_mfma_f32_16x16x32_bf16 a[56:59], a[136:139], v[124:127], a[56:59]\n\t"
"ds_read_b64_tr_b16 a[164:165], v13 offset:8192\n\t"
"v_mfma_f32_16x16x32_bf16 a[56:59], a[152:155], v[148:151], a[56:59]\n\t"
"v_mfma_f32_16x16x32_bf16 v[80:83], a[156:159], v[148:151], v[80:83]\n\t"
"ds_read_b64_tr_b16 a[166:167], v13 offset:9216\n\t"
"v_mfma_f32_16x16x32_bf16 v[80:83], a[140:143], v[124:127], v[80:83]\n\t"
"ds_read_b64_tr_b16 a[168:169], v14 offset:8192\n\t"
"v_mfma_f32_16x16x32_bf16 v[84:87], a[140:143], v[152:155], v[84:87]\n\t"
"ds_read_b64_tr_b16 a[170:171], v14 offset:9216\n\t"
"v_mfma_f32_16x16x32_bf16 v[84:87], a[156:159], v[156:159], v[84:87]\n\t"
"buffer_load_dwordx4 v5, s[12:15], s41 offen offset:1024 lds\n\t"
"v_mfma_f32_16x16x32_bf16 a[68:71], a[152:155], v[156:159], a[68:71]\n\t"
"ds_read_b64_tr_b16 a[172:173], v15 offset:8192\n\t"
"v_mfma_f32_16x16x32_bf16 a[68:71], a[136:139], v[152:155], a[68:71]\n\t"
"ds_read_b64_tr_b16 a[174:175], v15 offset:9216\n\t"
"v_mov_b32_dpp v164, v112 row_shr:12 row_mask:0xf bank_mask:0x8\n\t"
"v_mfma_f32_16x16x32_bf16 a[64:67], a[132:135], v[152:155], a[64:67]\n\t"
"s_waitcnt vmcnt(23)\n\t"
"ds_read_b64_tr_b16 a[176:177], v12 offset:16384\n\t"
"v_mfma_f32_16x16x32_bf16 a[64:67], a[148:151], v[156:159], a[64:67]\n\t"
"v_mov_b32_dpp v165, v113 row_shr:12 row_mask:0xf bank_mask:0x8\n\t"
"v_mfma_f32_16x16x32_bf16 a[60:63], a[144:147], v[156:159], a[60:63]\n\t"
"ds_read_b64_tr_b16 a[178:179], v12 offset:17408\n\t"
"v_mfma_f32_16x16x32_bf16 a[60:63], a[128:131], v[152:155], a[60:63]\n\t"
"v_mov_b32_dpp v166, v114 row_shr:12 row_mask:0xf bank_mask:0x8\n\t"
"v_mfma_f32_16x16x32_bf16 a[72:75], a[128:131], v[160:163], a[72:75]\n\t"
"ds_read_b64_tr_b16 a[180:181], v13 offset:16384\n\t"
"v_mfma_f32_16x16x32_bf16 a[72:75], a[144:147], v[132:135], a[72:75]\n\t"
"buffer_load_dwordx4 v6, s[12:15], s41 offen offset:2048 lds\n\t"
"v_mfma_f32_16x16x32_bf16 a[76:79], a[148:151], v[132:135], a[76:79]\n\t"
"ds_read_b64_tr_b16 a[182:183], v13 offset:17408\n\t"
"v_mfma_f32_16x16x32_bf16 a[76:79], a[132:135], v[160:163], a[76:79]\n\t"
"v_mov_b32_dpp v167, v115 row_shr:12 row_mask:0xf bank_mask:0x8\n\t"
"v_mfma_f32_16x16x32_bf16 a[80:83], a[136:139], v[160:163], a[80:83]\n\t"
"ds_read_b64_tr_b16 a[184:185], v14 offset:16384\n\t"
"v_mfma_f32_16x16x32_bf16 a[80:83], a[152:155], v[132:135], a[80:83]\n\t"
"v_mfma_f32_16x16x32_bf16 v[88:91], a[156:159], v[132:135], v[88:91]\n\t"
"ds_read_b64_tr_b16 a[186:187], v14 offset:17408\n\t"
"v_mfma_f32_16x16x32_bf16 v[88:91], a[140:143], v[160:163], v[88:91]\n\t"
"ds_read_b64_tr_b16 a[188:189], v15 offset:16384\n\t"
"v_mov_b32_dpp v164, v40 row_shl:1 row_mask:0xf bank_mask:0xf\n\t"
"v_mfma_f32_16x16x32_bf16 v[92:95], a[140:143], v[136:139], v[92:95]\n\t"
"ds_read_b64_tr_b16 a[190:191], v15 offset:17408\n\t"
"v_mfma_f32_16x16x32_bf16 v[92:95], a[156:159], v[140:143], v[92:95]\n\t"
"buffer_load_dwordx4 v7, s[12:15], s41 offen offset:3072 lds\n\t"
"v_mfma_f32_16x16x32_bf16 a[92:95], a[152:155], v[140:143], a[92:95]\n\t"
"v_mov_b32_dpp v165, v41 row_shl:1 row_mask:0xf bank_mask:0xf\n\t"
"v_mfma_f32_16x16x32_bf16 a[92:95], a[136:139], v[136:139], a[92:95]\n\t"
"v_mov_b32_dpp v166, v42 row_shl:1 row_mask:0xf bank_mask:0xf\n\t"
"v_mfma_f32_16x16x32_bf16 a[88:91], a[132:135], v[136:139], a[88:91]\n\t"
"v_mov_b32_dpp v167, v43 row_shl:1 row_mask:0xf bank_mask:0xf\n\t"
"v_mfma_f32_16x16x32_bf16 a[88:91], a[148:151], v[140:143], a[88:91]\n\t"
"v_mfma_f32_16x16x32_bf16 a[84:87], a[144:147], v[140:143], a[84:87]\n\t"
"v_mfma_f32_16x16x32_bf16 a[84:87], a[128:131], v[136:139], a[84:87]\n\t"
"s_waitcnt lgkmcnt(0)\n\t"
"s_cmp_eq_u32 s38, 15\n\t"
"s_cselect_b32 s28, 0x03ff03ff, -1\n\t"
"s_cselect_b32 s30, 0xfe00fe00, 0\n\t"
"s_mov_b32 s39, 0x3c000000\n\t"
"s_cselect_b32 s39, 0x3c607038, s39\n\t"
"v_mfma_f32_16x16x32_bf16 a[0:3], a[160:163], v[48:51], a[0:3]\n\t"
"v_mfma_f32_16x16x32_bf16 a[0:3], a[176:179], v[52:55], a[0:3]\n\t"
"v_mfma_f32_16x16x32_bf16 a[4:7], a[180:183], v[52:55], a[4:7]\n\t"
"s_mov_b32 s29, s28\n\t"
"s_mov_b32 s31, s30\n\t"
"v_mfma_f32_16x16x32_bf16 a[4:7], a[164:167], v[48:51], a[4:7]\n\t"
"s_mov_b32 s36, 0x11111111\n\t"
"s_mov_b32 s37, 0x11111111\n\t"
"v_mfma_f32_16x16x32_bf16 a[8:11], a[168:171], v[48:51], a[8:11]\n\t"
"s_mov_b64 s[4:5], -1\n\t"
"v_mfma_f32_16x16x32_bf16 a[8:11], a[184:187], v[52:55], a[8:11]\n\t"
"v_mfma_f32_16x16x32_bf16 v[64:67], a[188:191], v[52:55], v[64:67]\n\t"
"v_mfma_f32_16x16x32_bf16 v[64:67], a[172:175], v[48:51], v[64:67]\n\t"
"v_mfma_f32_16x16x32_bf16 v[68:71], a[172:175], v[56:59], v[68:71]\n\t"
"v_mfma_f32_16x16x32_bf16 v[68:71], a[188:191], v[60:63], v[68:71]\n\t"
"v_mfma_f32_16x16x32_bf16 a[20:23], a[184:187], v[60:63], a[20:23]\n\t"
"v_mfma_f32_16x16x32_bf16 a[20:23], a[168:171], v[56:59], a[20:23]\n\t"
"v_mov_b32_dpp v168, v108 row_shr:11 row_mask:0xf bank_mask:0x8\n\t"
"v_mfma_f32_16x16x32_bf16 a[16:19], a[164:167], v[56:59], a[16:19]\n\t"
"v_mfma_f32_16x16x32_bf16 a[16:19], a[180:183], v[60:63], a[16:19]\n\t"
"v_mov_b32_dpp v169, v109 row_shr:11 row_mask:0xf bank_mask:0x8\n\t"
"v_mfma_f32_16x16x32_bf16 a[12:15], a[176:179], v[60:63], a[12:15]\n\t"
"v_mfma_f32_16x16x32_bf16 a[12:15], a[160:163], v[56:59], a[12:15]\n\t"
"v_mov_b32_dpp v170, v110 row_shr:11 row_mask:0xf bank_mask:0x8\n\t"
"v_mfma_f32_16x16x32_bf16 a[24:27], a[160:163], v[116:119], a[24:27]\n\t"
"v_mfma_f32_16x16x32_bf16 a[24:27], a[176:179], v[120:123], a[24:27]\n\t"
"v_mfma_f32_16x16x32_bf16 a[28:31], a[180:183], v[120:123], a[28:31]\n\t"
"v_mfma_f32_16x16x32_bf16 a[28:31], a[164:167], v[116:119], a[28:31]\n\t"
"v_mov_b32_dpp v171, v111 row_shr:11 row_mask:0xf bank_mask:0x8\n\t"
"v_mfma_f32_16x16x32_bf16 a[32:35], a[168:171], v[116:119], a[32:35]\n\t"
"v_mfma_f32_16x16x32_bf16 a[32:35], a[184:187], v[120:123], a[32:35]\n\t"
"v_mfma_f32_16x16x32_bf16 v[72:75], a[188:191], v[120:123], v[72:75]\n\t"
"v_mfma_f32_16x16x32_bf16 v[72:75], a[172:175], v[116:119], v[72:75]\n\t"
"v_mov_b32_dpp v168, v48 row_shl:1 row_mask:0xf bank_mask:0xf\n\t"
"v_mfma_f32_16x16x32_bf16 v[76:79], a[172:175], v[124:127], v[76:79]\n\t"
"v_mfma_f32_16x16x32_bf16 v[76:79], a[188:191], v[148:151], v[76:79]\n\t"
"v_mfma_f32_16x16x32_bf16 a[44:47], a[184:187], v[148:151], a[44:47]\n\t"
"v_mov_b32_dpp v169, v49 row_shl:1 row_mask:0xf bank_mask:0xf\n\t"
"v_mfma_f32_16x16x32_bf16 a[44:47], a[168:171], v[124:127], a[44:47]\n\t"
"v_mov_b32_dpp v170, v50 row_shl:1 row_mask:0xf bank_mask:0xf\n\t"
"v_mfma_f32_16x16x32_bf16 a[40:43], a[164:167], v[124:127], a[40:43]\n\t"
"v_mov_b32_dpp v171, v51 row_shl:1 row_mask:0xf bank_mask:0xf\n\t"
"v_mfma_f32_16x16x32_bf16 a[40:43], a[180:183], v[148:151], a[40:43]\n\t"
"v_mfma_f32_16x16x32_bf16 a[36:39], a[176:179], v[148:151], a[36:39]\n\t"
"v_mfma_f32_16x16x32_bf16 a[36:39], a[160:163], v[124:127], a[36:39]\n\t"
"v_mfma_f32_16x16x32_bf16 a[48:51], a[160:163], v[152:155], a[48:51]\n\t"
"s_add_u32 m0, s26, 20480\n\t"
"s_add_u32 s42, s24, 1024\n\t"
"v_mfma_f32_16x16x32_bf16 a[48:51], a[176:179], v[156:159], a[48:51]\n\t"
"buffer_load_dwordx4 v8, s[8:11], s42 offen lds\n\t"
"v_mfma_f32_16x16x32_bf16 a[52:55], a[180:183], v[156:159], a[52:55]\n\t"
"s_waitcnt vmcnt(22)\n\t"
"ds_read_b64_tr_b16 a[192:193], v12 offset:32768\n\t"
"v_mfma_f32_16x16x32_bf16 a[52:55], a[164:167], v[152:155], a[52:55]\n\t"
"ds_read_b64_tr_b16 a[194:195], v12 offset:33792\n\t"
"v_mfma_f32_16x16x32_bf16 a[56:59], a[168:171], v[152:155], a[56:59]\n\t"
"ds_read_b64_tr_b16 a[196:197], v13 offset:32768\n\t"
"v_mfma_f32_16x16x32_bf16 a[56:59], a[184:187], v[156:159], a[56:59]\n\t"
"v_mfma_f32_16x16x32_bf16 v[80:83], a[188:191], v[156:159], v[80:83]\n\t"
"ds_read_b64_tr_b16 a[198:199], v13 offset:33792\n\t"
"v_mfma_f32_16x16x32_bf16 v[80:83], a[172:175], v[152:155], v[80:83]\n\t"
"ds_read_b64_tr_b16 a[200:201], v14 offset:32768\n\t"
"v_mfma_f32_16x16x32_bf16 v[84:87], a[172:175], v[160:163], v[84:87]\n\t"
"ds_read_b64_tr_b16 a[202:203], v14 offset:33792\n\t"
"v_mfma_f32_16x16x32_bf16 v[84:87], a[188:191], v[132:135], v[84:87]\n\t"
"buffer_load_dwordx4 v9, s[8:11], s42 offen offset:1024 lds\n\t"
"v_mfma_f32_16x16x32_bf16 a[68:71], a[184:187], v[132:135], a[68:71]\n\t"
"ds_read_b64_tr_b16 a[204:205], v15 offset:32768\n\t"
"v_mfma_f32_16x16x32_bf16 a[68:71], a[168:171], v[160:163], a[68:71]\n\t"
"ds_read_b64_tr_b16 a[206:207], v15 offset:33792\n\t"
"v_mov_b32_dpp v172, v112 row_shr:11 row_mask:0xf bank_mask:0x8\n\t"
"v_mfma_f32_16x16x32_bf16 a[64:67], a[164:167], v[160:163], a[64:67]\n\t"
"s_waitcnt vmcnt(19)\n\t"
"ds_read_b64_tr_b16 a[208:209], v12 offset:36864\n\t"
"v_mfma_f32_16x16x32_bf16 a[64:67], a[180:183], v[132:135], a[64:67]\n\t"
"v_mov_b32_dpp v173, v113 row_shr:11 row_mask:0xf bank_mask:0x8\n\t"
"v_mfma_f32_16x16x32_bf16 a[60:63], a[176:179], v[132:135], a[60:63]\n\t"
"ds_read_b64_tr_b16 a[210:211], v12 offset:37888\n\t"
"v_mfma_f32_16x16x32_bf16 a[60:63], a[160:163], v[160:163], a[60:63]\n\t"
"v_mov_b32_dpp v174, v114 row_shr:11 row_mask:0xf bank_mask:0x8\n\t"
"v_mfma_f32_16x16x32_bf16 a[72:75], a[160:163], v[136:139], a[72:75]\n\t"
"ds_read_b64_tr_b16 a[212:213], v13 offset:36864\n\t"
"v_mfma_f32_16x16x32_bf16 a[72:75], a[176:179], v[140:143], a[72:75]\n\t"
"buffer_load_dwordx4 v8, s[8:11], s42 offen offset:2048 lds\n\t"
"v_mfma_f32_16x16x32_bf16 a[76:79], a[180:183], v[140:143], a[76:79]\n\t"
"ds_read_b64_tr_b16 a[214:215], v13 offset:37888\n\t"
"v_mfma_f32_16x16x32_bf16 a[76:79], a[164:167], v[136:139], a[76:79]\n\t"
"v_mov_b32_dpp v175, v115 row_shr:11 row_mask:0xf bank_mask:0x8\n\t"
"v_mfma_f32_16x16x32_bf16 a[80:83], a[168:171], v[136:139], a[80:83]\n\t"
"ds_read_b64_tr_b16 a[216:217], v14 offset:36864\n\t"
"v_mfma_f32_16x16x32_bf16 a[80:83], a[184:187], v[140:143], a[80:83]\n\t"
"v_mfma_f32_16x16x32_bf16 v[88:91], a[188:191], v[140:143], v[88:91]\n\t"
"ds_read_b64_tr_b16 a[218:219], v14 offset:37888\n\t"
"v_mfma_f32_16x16x32_bf16 v[88:91], a[172:175], v[136:139], v[88:91]\n\t"
"ds_read_b64_tr_b16 a[220:221], v15 offset:36864\n\t"
"v_mov_b32_dpp v172, v52 row_shl:1 row_mask:0xf bank_mask:0xf\n\t"
"v_mfma_f32_16x16x32_bf16 v[92:95], a[172:175], v[144:147], v[92:95]\n\t"
"ds_read_b64_tr_b16 a[222:223], v15 offset:37888\n\t"
"v_mfma_f32_16x16x32_bf16 v[92:95], a[188:191], v[164:167], v[92:95]\n\t"
"buffer_load_dwordx4 v9, s[8:11], s42 offen offset:3072 lds\n\t"
"v_mfma_f32_16x16x32_bf16 a[92:95], a[184:187], v[164:167], a[92:95]\n\t"
"v_mov_b32_dpp v173, v53 row_shl:1 row_mask:0xf bank_mask:0xf\n\t"
"v_mfma_f32_16x16x32_bf16 a[92:95], a[168:171], v[144:147], a[92:95]\n\t"
"v_mov_b32_dpp v174, v54 row_shl:1 row_mask:0xf bank_mask:0xf\n\t"
"v_mfma_f32_16x16x32_bf16 a[88:91], a[164:167], v[144:147], a[88:91]\n\t"
"v_mov_b32_dpp v175, v55 row_shl:1 row_mask:0xf bank_mask:0xf\n\t"
"v_mfma_f32_16x16x32_bf16 a[88:91], a[180:183], v[164:167], a[88:91]\n\t"
"v_mfma_f32_16x16x32_bf16 a[84:87], a[176:179], v[164:167], a[84:87]\n\t"
"v_mfma_f32_16x16x32_bf16 a[84:87], a[160:163], v[144:147], a[84:87]\n\t"
"s_waitcnt lgkmcnt(0)\n\t"
"v_mfma_f32_16x16x32_bf16 a[0:3], a[192:195], v[56:59], a[0:3]\n\t"
"s_add_u32 m0, s26, 4096\n\t"
"s_add_u32 s40, s24, 5120\n\t"
"v_mfma_f32_16x16x32_bf16 a[0:3], a[208:211], v[60:63], a[0:3]\n\t"
"buffer_load_dwordx4 v8, s[8:11], s40 offen lds\n\t"
"v_mfma_f32_16x16x32_bf16 a[4:7], a[212:215], v[60:63], a[4:7]\n\t"
"v_mfma_f32_16x16x32_bf16 a[4:7], a[196:199], v[56:59], a[4:7]\n\t"
"v_mfma_f32_16x16x32_bf16 a[8:11], a[200:203], v[56:59], a[8:11]\n\t"
"v_mfma_f32_16x16x32_bf16 a[8:11], a[216:219], v[60:63], a[8:11]\n\t"
"buffer_load_dwordx4 v9, s[8:11], s40 offen offset:1024 lds\n\t"
"v_mfma_f32_16x16x32_bf16 v[64:67], a[220:223], v[60:63], v[64:67]\n\t"
"v_mfma_f32_16x16x32_bf16 v[64:67], a[204:207], v[56:59], v[64:67]\n\t"
"v_mfma_f32_16x16x32_bf16 v[68:71], a[204:207], v[116:119], v[68:71]\n\t"
"v_mfma_f32_16x16x32_bf16 v[68:71], a[220:223], v[120:123], v[68:71]\n\t"
"buffer_load_dwordx4 v8, s[8:11], s40 offen offset:2048 lds\n\t"
"v_mfma_f32_16x16x32_bf16 a[20:23], a[216:219], v[120:123], a[20:23]\n\t"
"v_mfma_f32_16x16x32_bf16 a[20:23], a[200:203], v[116:119], a[20:23]\n\t"
"v_mov_b32_dpp v180, v108 row_shr:10 row_mask:0xf bank_mask:0x8\n\t"
"v_mfma_f32_16x16x32_bf16 a[16:19], a[196:199], v[116:119], a[16:19]\n\t"
"v_mfma_f32_16x16x32_bf16 a[16:19], a[212:215], v[120:123], a[16:19]\n\t"
"buffer_load_dwordx4 v9, s[8:11], s40 offen offset:3072 lds\n\t"
"v_mov_b32_dpp v181, v109 row_shr:10 row_mask:0xf bank_mask:0x8\n\t"
"v_mfma_f32_16x16x32_bf16 a[12:15], a[208:211], v[120:123], a[12:15]\n\t"
"v_mfma_f32_16x16x32_bf16 a[12:15], a[192:195], v[116:119], a[12:15]\n\t"
"v_mov_b32_dpp v182, v110 row_shr:10 row_mask:0xf bank_mask:0x8\n\t"
"v_mfma_f32_16x16x32_bf16 a[24:27], a[192:195], v[124:127], a[24:27]\n\t"
"s_add_u32 m0, s26, 12288\n\t"
"s_add_u32 s41, s24, 9216\n\t"
"v_mfma_f32_16x16x32_bf16 a[24:27], a[208:211], v[148:151], a[24:27]\n\t"
"buffer_load_dwordx4 v8, s[8:11], s41 offen lds\n\t"
"v_mfma_f32_16x16x32_bf16 a[28:31], a[212:215], v[148:151], a[28:31]\n\t"
"v_mfma_f32_16x16x32_bf16 a[28:31], a[196:199], v[124:127], a[28:31]\n\t"
"v_mov_b32_dpp v183, v111 row_shr:10 row_mask:0xf bank_mask:0x8\n\t"
"v_mfma_f32_16x16x32_bf16 a[32:35], a[200:203], v[124:127], a[32:35]\n\t"
"v_mfma_f32_16x16x32_bf16 a[32:35], a[216:219], v[148:151], a[32:35]\n\t"
"buffer_load_dwordx4 v9, s[8:11], s41 offen offset:1024 lds\n\t"
"v_mfma_f32_16x16x32_bf16 v[72:75], a[220:223], v[148:151], v[72:75]\n\t"
"v_mfma_f32_16x16x32_bf16 v[72:75], a[204:207], v[124:127], v[72:75]\n\t"
"v_mov_b32_dpp v180, v56 row_shl:1 row_mask:0xf bank_mask:0xf\n\t"
"v_mfma_f32_16x16x32_bf16 v[76:79], a[204:207], v[152:155], v[76:79]\n\t"
"v_mfma_f32_16x16x32_bf16 v[76:79], a[220:223], v[156:159], v[76:79]\n\t"
"buffer_load_dwordx4 v8, s[8:11], s41 offen offset:2048 lds\n\t"
"v_mfma_f32_16x16x32_bf16 a[44:47], a[216:219], v[156:159], a[44:47]\n\t"
"v_mov_b32_dpp v181, v57 row_shl:1 row_mask:0xf bank_mask:0xf\n\t"
"v_mfma_f32_16x16x32_bf16 a[44:47], a[200:203], v[152:155], a[44:47]\n\t"
"v_mov_b32_dpp v182, v58 row_shl:1 row_mask:0xf bank_mask:0xf\n\t"
"v_mfma_f32_16x16x32_bf16 a[40:43], a[196:199], v[152:155], a[40:43]\n\t"
"v_mov_b32_dpp v183, v59 row_shl:1 row_mask:0xf bank_mask:0xf\n\t"
"v_mfma_f32_16x16x32_bf16 a[40:43], a[212:215], v[156:159], a[40:43]\n\t"
"buffer_load_dwordx4 v9, s[8:11], s41 offen offset:3072 lds\n\t"
"v_mfma_f32_16x16x32_bf16 a[36:39], a[208:211], v[156:159], a[36:39]\n\t"
"v_mfma_f32_16x16x32_bf16 a[36:39], a[192:195], v[152:155], a[36:39]\n\t"
"v_mfma_f32_16x16x32_bf16 a[48:51], a[192:195], v[160:163], a[48:51]\n\t"
"s_add_u32 m0, s26, 8192\n\t"
"s_add_u32 s42, s24, 13312\n\t"
"v_mfma_f32_16x16x32_bf16 a[48:51], a[208:211], v[132:135], a[48:51]\n\t"
"buffer_load_dwordx4 v8, s[8:11], s42 offen lds\n\t"
"v_mfma_f32_16x16x32_bf16 a[52:55], a[212:215], v[132:135], a[52:55]\n\t"
"s_waitcnt vmcnt(26)\n\t"
"ds_read_b64_tr_b16 a[224:225], v12 offset:28672\n\t"
"s_waitcnt vmcnt(9)\n\t"
"ds_read_b128 v[188:191], v16 offset:20480\n\t"
"v_mfma_f32_16x16x32_bf16 a[52:55], a[196:199], v[160:163], a[52:55]\n\t"
"ds_read_b64_tr_b16 a[226:227], v12 offset:29696\n\t"
"v_mfma_f32_16x16x32_bf16 a[56:59], a[200:203], v[160:163], a[56:59]\n\t"
"ds_read_b64_tr_b16 a[228:229], v13 offset:28672\n\t"
"ds_read_b128 v[192:195], v17 offset:20480\n\t"
"v_mfma_f32_16x16x32_bf16 a[56:59], a[216:219], v[132:135], a[56:59]\n\t"
"buffer_load_dwordx4 v9, s[8:11], s42 offen offset:1024 lds\n\t"
"v_mfma_f32_16x16x32_bf16 v[80:83], a[220:223], v[132:135], v[80:83]\n\t"
"ds_read_b64_tr_b16 a[230:231], v13 offset:29696\n\t"
"ds_read_b128 v[204:207], v16 offset:22528\n\t"
"v_mfma_f32_16x16x32_bf16 v[80:83], a[204:207], v[160:163], v[80:83]\n\t"
"ds_read_b64_tr_b16 a[232:233], v14 offset:28672\n\t"
"v_mfma_f32_16x16x32_bf16 v[84:87], a[204:207], v[136:139], v[84:87]\n\t"
"ds_read_b64_tr_b16 a[234:235], v14 offset:29696\n\t"
"ds_read_b128 v[212:215], v17 offset:22528\n\t"
"v_mfma_f32_16x16x32_bf16 v[84:87], a[220:223], v[140:143], v[84:87]\n\t"
"buffer_load_dwordx4 v8, s[8:11], s42 offen offset:2048 lds\n\t"
"v_mfma_f32_16x16x32_bf16 a[68:71], a[216:219], v[140:143], a[68:71]\n\t"
"ds_read_b64_tr_b16 a[236:237], v15 offset:28672\n\t"
"v_mfma_f32_16x16x32_bf16 a[68:71], a[200:203], v[136:139], a[68:71]\n\t"
"ds_read_b64_tr_b16 a[238:239], v15 offset:29696\n\t"
"v_mov_b32_dpp v220, v112 row_shr:10 row_mask:0xf bank_mask:0x8\n\t"
"v_mfma_f32_16x16x32_bf16 a[64:67], a[196:199], v[136:139], a[64:67]\n\t"
"ds_read_b64_tr_b16 a[240:241], v12 offset:0\n\t"
"v_mfma_f32_16x16x32_bf16 a[64:67], a[212:215], v[140:143], a[64:67]\n\t"
"buffer_load_dwordx4 v9, s[8:11], s42 offen offset:3072 lds\n\t"
"v_mov_b32_dpp v221, v113 row_shr:10 row_mask:0xf bank_mask:0x8\n\t"
"v_mfma_f32_16x16x32_bf16 a[60:63], a[208:211], v[140:143], a[60:63]\n\t"
"ds_read_b64_tr_b16 a[242:243], v12 offset:1024\n\t"
"v_mfma_f32_16x16x32_bf16 a[60:63], a[192:195], v[136:139], a[60:63]\n\t"
"v_mov_b32_dpp v222, v114 row_shr:10 row_mask:0xf bank_mask:0x8\n\t"
"v_mfma_f32_16x16x32_bf16 a[72:75], a[192:195], v[144:147], a[72:75]\n\t"
"s_add_u32 m0, s26, 16384\n\t"
"s_add_u32 s40, s6, 7372800\n\t"
"ds_read_b64_tr_b16 a[244:245], v13 offset:0\n\t"
"v_mfma_f32_16x16x32_bf16 a[72:75], a[208:211], v[164:167], a[72:75]\n\t"
"buffer_load_dwordx4 v4, s[12:15], s40 offen lds\n\t"
"v_mfma_f32_16x16x32_bf16 a[76:79], a[212:215], v[164:167], a[76:79]\n\t"
"ds_read_b64_tr_b16 a[246:247], v13 offset:1024\n\t"
"v_mfma_f32_16x16x32_bf16 a[76:79], a[196:199], v[144:147], a[76:79]\n\t"
"v_mov_b32_dpp v223, v115 row_shr:10 row_mask:0xf bank_mask:0x8\n\t"
"v_mfma_f32_16x16x32_bf16 a[80:83], a[200:203], v[144:147], a[80:83]\n\t"
"ds_read_b64_tr_b16 a[248:249], v14 offset:0\n\t"
"v_mfma_f32_16x16x32_bf16 a[80:83], a[216:219], v[164:167], a[80:83]\n\t"
"buffer_load_dwordx4 v5, s[12:15], s40 offen offset:1024 lds\n\t"
"v_mfma_f32_16x16x32_bf16 v[88:91], a[220:223], v[164:167], v[88:91]\n\t"
"ds_read_b64_tr_b16 a[250:251], v14 offset:1024\n\t"
"v_mfma_f32_16x16x32_bf16 v[88:91], a[204:207], v[144:147], v[88:91]\n\t"
"ds_read_b64_tr_b16 a[252:253], v15 offset:0\n\t"
"v_mov_b32_dpp v220, v60 row_shl:1 row_mask:0xf bank_mask:0xf\n\t"
"v_mfma_f32_16x16x32_bf16 v[92:95], a[204:207], v[168:171], v[92:95]\n\t"
"ds_read_b64_tr_b16 a[254:255], v15 offset:1024\n\t"
"v_mfma_f32_16x16x32_bf16 v[92:95], a[220:223], v[172:175], v[92:95]\n\t"
"buffer_load_dwordx4 v6, s[12:15], s40 offen offset:2048 lds\n\t"
"v_mfma_f32_16x16x32_bf16 a[92:95], a[216:219], v[172:175], a[92:95]\n\t"
"v_mov_b32_dpp v221, v61 row_shl:1 row_mask:0xf bank_mask:0xf\n\t"
"v_mfma_f32_16x16x32_bf16 a[92:95], a[200:203], v[168:171], a[92:95]\n\t"
"v_mov_b32_dpp v222, v62 row_shl:1 row_mask:0xf bank_mask:0xf\n\t"
"v_mfma_f32_16x16x32_bf16 a[88:91], a[196:199], v[168:171], a[88:91]\n\t"
"v_mov_b32_dpp v223, v63 row_shl:1 row_mask:0xf bank_mask:0xf\n\t"
"v_mfma_f32_16x16x32_bf16 a[88:91], a[212:215], v[172:175], a[88:91]\n\t"
"buffer_load_dwordx4 v7, s[12:15], s40 offen offset:3072 lds\n\t"
"v_mfma_f32_16x16x32_bf16 a[84:87], a[208:211], v[172:175], a[84:87]\n\t"
"v_mfma_f32_16x16x32_bf16 a[84:87], a[192:195], v[168:171], a[84:87]\n\t"
"s_waitcnt lgkmcnt(0)\n\t"
"s_waitcnt vmcnt(12)\n\t"
"ds_read_b128 v[44:47], v16 offset:4096\n\t"
"v_mfma_f32_16x16x32_bf16 a[0:3], a[224:227], v[116:119], a[0:3]\n\t"
"s_add_u32 m0, s26, 32768\n\t"
"s_add_u32 s41, s25, 524288\n\t"
"v_mfma_f32_16x16x32_bf16 a[0:3], a[240:243], v[120:123], a[0:3]\n\t"
"buffer_load_dwordx4 v4, s[12:15], s41 offen lds\n\t"
"v_mfma_f32_16x16x32_bf16 a[4:7], a[244:247], v[120:123], a[4:7]\n\t"
"s_waitcnt vmcnt(9)\n\t"
"ds_read_b128 v[200:203], v16 offset:12288\n\t"
"v_mfma_f32_16x16x32_bf16 a[4:7], a[228:231], v[116:119], a[4:7]\n\t"
"ds_read_b128 v[176:179], v17 offset:4096\n\t"
"v_mfma_f32_16x16x32_bf16 a[8:11], a[232:235], v[116:119], a[8:11]\n\t"
"buffer_load_dwordx4 v5, s[12:15], s41 offen offset:1024 lds\n\t"
"ds_read_b128 v[208:211], v17 offset:12288\n\t"
"v_mfma_f32_16x16x32_bf16 a[8:11], a[248:251], v[120:123], a[8:11]\n\t"
"ds_read_b128 v[184:187], v16 offset:6144\n\t"
"v_mfma_f32_16x16x32_bf16 v[64:67], a[252:255], v[120:123], v[64:67]\n\t"
"ds_read_b128 v[216:219], v16 offset:14336\n\t"
"v_mfma_f32_16x16x32_bf16 v[64:67], a[236:239], v[116:119], v[64:67]\n\t"
"buffer_load_dwordx4 v6, s[12:15], s41 offen offset:2048 lds\n\t"
"ds_read_b128 v[196:199], v17 offset:6144\n\t"
"v_mfma_f32_16x16x32_bf16 v[68:71], a[236:239], v[124:127], v[68:71]\n\t"
"ds_read_b128 v[224:227], v17 offset:14336\n\t"
"v_mfma_f32_16x16x32_bf16 v[68:71], a[252:255], v[148:151], v[68:71]\n\t"
"v_mfma_f32_16x16x32_bf16 a[20:23], a[248:251], v[148:151], a[20:23]\n\t"
"buffer_load_dwordx4 v7, s[12:15], s41 offen offset:3072 lds\n\t"
"v_mfma_f32_16x16x32_bf16 a[20:23], a[232:235], v[124:127], a[20:23]\n\t"
"v_mov_b32_dpp v128, v108 row_shr:9 row_mask:0xf bank_mask:0x8\n\t"
"v_mfma_f32_16x16x32_bf16 a[16:19], a[228:231], v[124:127], a[16:19]\n\t"
"v_mfma_f32_16x16x32_bf16 a[16:19], a[244:247], v[148:151], a[16:19]\n\t"
"s_add_u32 m0, s26, 36864\n\t"
"s_add_u32 s42, s25, 557056\n\t"
"v_mov_b32_dpp v129, v109 row_shr:9 row_mask:0xf bank_mask:0x8\n\t"
"v_mfma_f32_16x16x32_bf16 a[12:15], a[240:243], v[148:151], a[12:15]\n\t"
"buffer_load_dwordx4 v4, s[12:15], s42 offen lds\n\t"
"v_mfma_f32_16x16x32_bf16 a[12:15], a[224:227], v[124:127], a[12:15]\n\t"
"v_mov_b32_dpp v130, v110 row_shr:9 row_mask:0xf bank_mask:0x8\n\t"
"v_mfma_f32_16x16x32_bf16 a[24:27], a[224:227], v[152:155], a[24:27]\n\t"
"v_mfma_f32_16x16x32_bf16 a[24:27], a[240:243], v[156:159], a[24:27]\n\t"
"buffer_load_dwordx4 v5, s[12:15], s42 offen offset:1024 lds\n\t"
"v_mfma_f32_16x16x32_bf16 a[28:31], a[244:247], v[156:159], a[28:31]\n\t"
"v_mfma_f32_16x16x32_bf16 a[28:31], a[228:231], v[152:155], a[28:31]\n\t"
"v_mov_b32_dpp v131, v111 row_shr:9 row_mask:0xf bank_mask:0x8\n\t"
"v_mfma_f32_16x16x32_bf16 a[32:35], a[232:235], v[152:155], a[32:35]\n\t"
"buffer_load_dwordx4 v6, s[12:15], s42 offen offset:2048 lds\n\t"
"v_mfma_f32_16x16x32_bf16 a[32:35], a[248:251], v[156:159], a[32:35]\n\t"
"v_mfma_f32_16x16x32_bf16 v[72:75], a[252:255], v[156:159], v[72:75]\n\t"
"v_mfma_f32_16x16x32_bf16 v[72:75], a[236:239], v[152:155], v[72:75]\n\t"
"buffer_load_dwordx4 v7, s[12:15], s42 offen offset:3072 lds\n\t"
"v_mov_b32_dpp v128, v116 row_shl:1 row_mask:0xf bank_mask:0xf\n\t"
"v_mfma_f32_16x16x32_bf16 v[76:79], a[236:239], v[160:163], v[76:79]\n\t"
"v_mfma_f32_16x16x32_bf16 v[76:79], a[252:255], v[132:135], v[76:79]\n\t"
"v_mfma_f32_16x16x32_bf16 a[44:47], a[248:251], v[132:135], a[44:47]\n\t"
"s_add_u32 m0, s26, 20480\n\t"
"s_add_u32 s40, s24, 263168\n\t"
"v_mov_b32_dpp v129, v117 row_shl:1 row_mask:0xf bank_mask:0xf\n\t"
"v_mfma_f32_16x16x32_bf16 a[44:47], a[232:235], v[160:163], a[44:47]\n\t"
"buffer_load_dwordx4 v10, s[8:11], s40 offen lds\n\t"
"v_mov_b32_dpp v130, v118 row_shl:1 row_mask:0xf bank_mask:0xf\n\t"
"v_mfma_f32_16x16x32_bf16 a[40:43], a[228:231], v[160:163], a[40:43]\n\t"
"v_mov_b32_dpp v131, v119 row_shl:1 row_mask:0xf bank_mask:0xf\n\t"
"v_mfma_f32_16x16x32_bf16 a[40:43], a[244:247], v[132:135], a[40:43]\n\t"
"v_mfma_f32_16x16x32_bf16 a[36:39], a[240:243], v[132:135], a[36:39]\n\t"
"v_mfma_f32_16x16x32_bf16 a[36:39], a[224:227], v[160:163], a[36:39]\n\t"
"s_waitcnt vmcnt(13)\n\t"
"ds_read_b128 v[228:231], v16 offset:8192\n\t"
"v_mfma_f32_16x16x32_bf16 a[48:51], a[224:227], v[136:139], a[48:51]\n\t"
"s_add_u32 m0, s26, 28672\n\t"
"s_add_u32 s41, s25, 1572864\n\t"
"v_mfma_f32_16x16x32_bf16 a[48:51], a[240:243], v[140:143], a[48:51]\n\t"
"buffer_load_dwordx4 v4, s[12:15], s41 offen lds\n\t"
"v_mfma_f32_16x16x32_bf16 a[52:55], a[244:247], v[140:143], a[52:55]\n\t"
"ds_read_b64_tr_b16 a[96:97], v12 offset:24576\n\t"
"v_mfma_f32_16x16x32_bf16 a[52:55], a[228:231], v[136:139], a[52:55]\n\t"
"ds_read_b64_tr_b16 a[98:99], v12 offset:25600\n\t"
"ds_read_b128 v[232:235], v17 offset:8192\n\t"
"v_mfma_f32_16x16x32_bf16 a[56:59], a[232:235], v[136:139], a[56:59]\n\t"
"ds_read_b64_tr_b16 a[100:101], v13 offset:24576\n\t"
"v_mfma_f32_16x16x32_bf16 a[56:59], a[248:251], v[140:143], a[56:59]\n\t"
"ds_read_b128 v[236:239], v16 offset:10240\n\t"
"v_mfma_f32_16x16x32_bf16 v[80:83], a[252:255], v[140:143], v[80:83]\n\t"
"ds_read_b64_tr_b16 a[102:103], v13 offset:25600\n\t"
"v_mfma_f32_16x16x32_bf16 v[80:83], a[236:239], v[136:139], v[80:83]\n\t"
"ds_read_b64_tr_b16 a[104:105], v14 offset:24576\n\t"
"ds_read_b128 v[240:243], v17 offset:10240\n\t"
"v_mfma_f32_16x16x32_bf16 v[84:87], a[236:239], v[144:147], v[84:87]\n\t"
"ds_read_b64_tr_b16 a[106:107], v14 offset:25600\n\t"
"v_mfma_f32_16x16x32_bf16 v[84:87], a[252:255], v[164:167], v[84:87]\n\t"
"buffer_load_dwordx4 v5, s[12:15], s41 offen offset:1024 lds\n\t"
"v_mfma_f32_16x16x32_bf16 a[68:71], a[248:251], v[164:167], a[68:71]\n\t"
"ds_read_b64_tr_b16 a[108:109], v15 offset:24576\n\t"
"v_mfma_f32_16x16x32_bf16 a[68:71], a[232:235], v[144:147], a[68:71]\n\t"
"ds_read_b64_tr_b16 a[110:111], v15 offset:25600\n\t"
"v_mov_b32_dpp v244, v112 row_shr:9 row_mask:0xf bank_mask:0x8\n\t"
"v_mfma_f32_16x16x32_bf16 a[64:67], a[228:231], v[144:147], a[64:67]\n\t"
"s_waitcnt vmcnt(11)\n\t"
"ds_read_b64_tr_b16 a[112:113], v12 offset:16384\n\t"
"v_mfma_f32_16x16x32_bf16 a[64:67], a[244:247], v[164:167], a[64:67]\n\t"
"v_mov_b32_dpp v245, v113 row_shr:9 row_mask:0xf bank_mask:0x8\n\t"
"v_mfma_f32_16x16x32_bf16 a[60:63], a[240:243], v[164:167], a[60:63]\n\t"
"ds_read_b64_tr_b16 a[114:115], v12 offset:17408\n\t"
"v_mfma_f32_16x16x32_bf16 a[60:63], a[224:227], v[144:147], a[60:63]\n\t"
"v_mov_b32_dpp v246, v114 row_shr:9 row_mask:0xf bank_mask:0x8\n\t"
"v_mfma_f32_16x16x32_bf16 a[72:75], a[224:227], v[168:171], a[72:75]\n\t"
"ds_read_b64_tr_b16 a[116:117], v13 offset:16384\n\t"
"v_mfma_f32_16x16x32_bf16 a[72:75], a[240:243], v[172:175], a[72:75]\n\t"
"buffer_load_dwordx4 v6, s[12:15], s41 offen offset:2048 lds\n\t"
"v_mfma_f32_16x16x32_bf16 a[76:79], a[244:247], v[172:175], a[76:79]\n\t"
"ds_read_b64_tr_b16 a[118:119], v13 offset:17408\n\t"
"v_mfma_f32_16x16x32_bf16 a[76:79], a[228:231], v[168:171], a[76:79]\n\t"
"v_mov_b32_dpp v247, v115 row_shr:9 row_mask:0xf bank_mask:0x8\n\t"
"v_mfma_f32_16x16x32_bf16 a[80:83], a[232:235], v[168:171], a[80:83]\n\t"
"ds_read_b64_tr_b16 a[120:121], v14 offset:16384\n\t"
"v_mfma_f32_16x16x32_bf16 a[80:83], a[248:251], v[172:175], a[80:83]\n\t"
"v_mfma_f32_16x16x32_bf16 v[88:91], a[252:255], v[172:175], v[88:91]\n\t"
"ds_read_b64_tr_b16 a[122:123], v14 offset:17408\n\t"
"v_mfma_f32_16x16x32_bf16 v[88:91], a[236:239], v[168:171], v[88:91]\n\t"
"ds_read_b64_tr_b16 a[124:125], v15 offset:16384\n\t"
"v_mov_b32_dpp v244, v120 row_shl:1 row_mask:0xf bank_mask:0xf\n\t"
"v_mfma_f32_16x16x32_bf16 v[92:95], a[236:239], v[180:183], v[92:95]\n\t"
"ds_read_b64_tr_b16 a[126:127], v15 offset:17408\n\t"
"v_mfma_f32_16x16x32_bf16 v[92:95], a[252:255], v[220:223], v[92:95]\n\t"
"buffer_load_dwordx4 v7, s[12:15], s41 offen offset:3072 lds\n\t"
"v_mfma_f32_16x16x32_bf16 a[92:95], a[248:251], v[220:223], a[92:95]\n\t"
"v_mov_b32_dpp v245, v121 row_shl:1 row_mask:0xf bank_mask:0xf\n\t"
"v_mfma_f32_16x16x32_bf16 a[92:95], a[232:235], v[180:183], a[92:95]\n\t"
"v_mov_b32_dpp v246, v122 row_shl:1 row_mask:0xf bank_mask:0xf\n\t"
"v_mfma_f32_16x16x32_bf16 a[88:91], a[228:231], v[180:183], a[88:91]\n\t"
"v_mov_b32_dpp v247, v123 row_shl:1 row_mask:0xf bank_mask:0xf\n\t"
"v_mfma_f32_16x16x32_bf16 a[88:91], a[244:247], v[220:223], a[88:91]\n\t"
"v_mfma_f32_16x16x32_bf16 a[84:87], a[240:243], v[220:223], a[84:87]\n\t"
"v_mfma_f32_16x16x32_bf16 a[84:87], a[224:227], v[180:183], a[84:87]\n\t"
"s_xor_b32 s43, s97, 2621440\n\t"
"buffer_load_dword v24, v11, s[12:15], s43 offen\n\t"
"s_xor_b32 s43, s33, 4718592\n\t"
"buffer_load_dword v24, v96, s[12:15], s43 offen\n\t"
"s_waitcnt lgkmcnt(0)\n\t"
"v_mfma_f32_16x16x32_bf16 a[0:3], a[96:99], v[124:127], a[0:3]\n\t"
"s_add_u32 m0, s26, 0\n\t"
"s_add_u32 s42, s25, 1605632\n\t"
"v_mfma_f32_16x16x32_bf16 a[0:3], a[112:115], v[148:151], a[0:3]\n\t"
"buffer_load_dwordx4 v4, s[12:15], s42 offen lds\n\t"
"v_mfma_f32_16x16x32_bf16 a[4:7], a[116:119], v[148:151], a[4:7]\n\t"
"v_mfma_f32_16x16x32_bf16 a[4:7], a[100:103], v[124:127], a[4:7]\n\t"
"v_mfma_f32_16x16x32_bf16 a[8:11], a[104:107], v[124:127], a[8:11]\n\t"
"v_mfma_f32_16x16x32_bf16 a[8:11], a[120:123], v[148:151], a[8:11]\n\t"
"v_mfma_f32_16x16x32_bf16 v[64:67], a[124:127], v[148:151], v[64:67]\n\t"
"v_mfma_f32_16x16x32_bf16 v[64:67], a[108:111], v[124:127], v[64:67]\n\t"
"v_mfma_f32_16x16x32_bf16 v[68:71], a[108:111], v[152:155], v[68:71]\n\t"
"v_mfma_f32_16x16x32_bf16 v[68:71], a[124:127], v[156:159], v[68:71]\n\t"
"buffer_load_dwordx4 v5, s[12:15], s42 offen offset:1024 lds\n\t"
"s_waitcnt vmcnt(8)\n\t"
"ds_read_b128 v[100:103], v16 offset:20480\n\t"
"v_mfma_f32_16x16x32_bf16 a[20:23], a[120:123], v[156:159], a[20:23]\n\t"
"v_mfma_f32_16x16x32_bf16 a[20:23], a[104:107], v[152:155], a[20:23]\n\t"
"ds_read_b128 v[104:107], v17 offset:20480\n\t"
"v_mfma_f32_16x16x32_bf16 a[16:19], a[100:103], v[152:155], a[16:19]\n\t"
"v_mfma_f32_16x16x32_bf16 a[16:19], a[116:119], v[156:159], a[16:19]\n\t"
"v_mfma_f32_16x16x32_bf16 a[12:15], a[112:115], v[156:159], a[12:15]\n\t"
"v_mfma_f32_16x16x32_bf16 a[12:15], a[96:99], v[152:155], a[12:15]\n\t"
"v_mfma_f32_16x16x32_bf16 a[24:27], a[96:99], v[160:163], a[24:27]\n\t"
"v_mfma_f32_16x16x32_bf16 a[24:27], a[112:115], v[132:135], a[24:27]\n\t"
"buffer_load_dwordx4 v6, s[12:15], s42 offen offset:2048 lds\n\t"
"v_mfma_f32_16x16x32_bf16 a[28:31], a[116:119], v[132:135], a[28:31]\n\t"
"v_mfma_f32_16x16x32_bf16 a[28:31], a[100:103], v[160:163], a[28:31]\n\t"
"v_mfma_f32_16x16x32_bf16 a[32:35], a[104:107], v[160:163], a[32:35]\n\t"
"v_mfma_f32_16x16x32_bf16 a[32:35], a[120:123], v[132:135], a[32:35]\n\t"
"v_mfma_f32_16x16x32_bf16 v[72:75], a[124:127], v[132:135], v[72:75]\n\t"
"v_mfma_f32_16x16x32_bf16 v[72:75], a[108:111], v[160:163], v[72:75]\n\t"
"v_mfma_f32_16x16x32_bf16 v[76:79], a[108:111], v[136:139], v[76:79]\n\t"
"v_mfma_f32_16x16x32_bf16 v[76:79], a[124:127], v[140:143], v[76:79]\n\t"
"buffer_load_dwordx4 v7, s[12:15], s42 offen offset:3072 lds\n\t"
"v_mfma_f32_16x16x32_bf16 a[44:47], a[120:123], v[140:143], a[44:47]\n\t"
"v_mfma_f32_16x16x32_bf16 a[44:47], a[104:107], v[136:139], a[44:47]\n\t"
"v_mfma_f32_16x16x32_bf16 a[40:43], a[100:103], v[136:139], a[40:43]\n\t"
"v_mfma_f32_16x16x32_bf16 a[40:43], a[116:119], v[140:143], a[40:43]\n\t"
"v_mfma_f32_16x16x32_bf16 a[36:39], a[112:115], v[140:143], a[36:39]\n\t"
"v_mfma_f32_16x16x32_bf16 a[36:39], a[96:99], v[136:139], a[36:39]\n\t"
"v_mfma_f32_16x16x32_bf16 a[48:51], a[96:99], v[144:147], a[48:51]\n\t"
"s_add_u32 m0, s26, 4096\n\t"
"s_add_u32 s40, s25, 2621440\n\t"
"v_mfma_f32_16x16x32_bf16 a[48:51], a[112:115], v[164:167], a[48:51]\n\t"
"buffer_load_dwordx4 v4, s[12:15], s40 offen lds\n\t"
"v_mfma_f32_16x16x32_bf16 a[52:55], a[116:119], v[164:167], a[52:55]\n\t"
"ds_read_b64_tr_b16 a[128:129], v12 offset:32768\n\t"
"v_mfma_f32_16x16x32_bf16 a[52:55], a[100:103], v[144:147], a[52:55]\n\t"
"ds_read_b64_tr_b16 a[130:131], v12 offset:33792\n\t"
"v_mfma_f32_16x16x32_bf16 a[56:59], a[104:107], v[144:147], a[56:59]\n\t"
"ds_read_b64_tr_b16 a[132:133], v13 offset:32768\n\t"
"v_mfma_f32_16x16x32_bf16 a[56:59], a[120:123], v[164:167], a[56:59]\n\t"
"v_mfma_f32_16x16x32_bf16 v[80:83], a[124:127], v[164:167], v[80:83]\n\t"
"ds_read_b64_tr_b16 a[134:135], v13 offset:33792\n\t"
"v_mfma_f32_16x16x32_bf16 v[80:83], a[108:111], v[144:147], v[80:83]\n\t"
"ds_read_b64_tr_b16 a[136:137], v14 offset:32768\n\t"
"v_mfma_f32_16x16x32_bf16 v[84:87], a[108:111], v[168:171], v[84:87]\n\t"
"ds_read_b64_tr_b16 a[138:139], v14 offset:33792\n\t"
"v_mfma_f32_16x16x32_bf16 v[84:87], a[124:127], v[172:175], v[84:87]\n\t"
"buffer_load_dwordx4 v5, s[12:15], s40 offen offset:1024 lds\n\t"
"v_mfma_f32_16x16x32_bf16 a[68:71], a[120:123], v[172:175], a[68:71]\n\t"
"ds_read_b64_tr_b16 a[140:141], v15 offset:32768\n\t"
"v_mfma_f32_16x16x32_bf16 a[68:71], a[104:107], v[168:171], a[68:71]\n\t"
"ds_read_b64_tr_b16 a[142:143], v15 offset:33792\n\t"
"v_mfma_f32_16x16x32_bf16 a[64:67], a[100:103], v[168:171], a[64:67]\n\t"
"ds_read_b64_tr_b16 a[144:145], v12 offset:36864\n\t"
"v_mfma_f32_16x16x32_bf16 a[64:67], a[116:119], v[172:175], a[64:67]\n\t"
"v_mfma_f32_16x16x32_bf16 a[60:63], a[112:115], v[172:175], a[60:63]\n\t"
"ds_read_b64_tr_b16 a[146:147], v12 offset:37888\n\t"
"v_mfma_f32_16x16x32_bf16 a[60:63], a[96:99], v[168:171], a[60:63]\n\t"
"v_mfma_f32_16x16x32_bf16 a[72:75], a[96:99], v[180:183], a[72:75]\n\t"
"ds_read_b64_tr_b16 a[148:149], v13 offset:36864\n\t"
"v_mfma_f32_16x16x32_bf16 a[72:75], a[112:115], v[220:223], a[72:75]\n\t"
"buffer_load_dwordx4 v6, s[12:15], s40 offen offset:2048 lds\n\t"
"v_mfma_f32_16x16x32_bf16 a[76:79], a[116:119], v[220:223], a[76:79]\n\t"
"ds_read_b64_tr_b16 a[150:151], v13 offset:37888\n\t"
"v_mfma_f32_16x16x32_bf16 a[76:79], a[100:103], v[180:183], a[76:79]\n\t"
"v_mfma_f32_16x16x32_bf16 a[80:83], a[104:107], v[180:183], a[80:83]\n\t"
"ds_read_b64_tr_b16 a[152:153], v14 offset:36864\n\t"
"v_mfma_f32_16x16x32_bf16 a[80:83], a[120:123], v[220:223], a[80:83]\n\t"
"v_mfma_f32_16x16x32_bf16 v[88:91], a[124:127], v[220:223], v[88:91]\n\t"
"ds_read_b64_tr_b16 a[154:155], v14 offset:37888\n\t"
"v_mfma_f32_16x16x32_bf16 v[88:91], a[108:111], v[180:183], v[88:91]\n\t"
"ds_read_b64_tr_b16 a[156:157], v15 offset:36864\n\t"
"v_mfma_f32_16x16x32_bf16 v[92:95], a[108:111], v[128:131], v[92:95]\n\t"
"ds_read_b64_tr_b16 a[158:159], v15 offset:37888\n\t"
"v_mfma_f32_16x16x32_bf16 v[92:95], a[124:127], v[244:247], v[92:95]\n\t"
"buffer_load_dwordx4 v7, s[12:15], s40 offen offset:3072 lds\n\t"
"v_mfma_f32_16x16x32_bf16 a[92:95], a[120:123], v[244:247], a[92:95]\n\t"
"v_mfma_f32_16x16x32_bf16 a[92:95], a[104:107], v[128:131], a[92:95]\n\t"
"v_mfma_f32_16x16x32_bf16 a[88:91], a[100:103], v[128:131], a[88:91]\n\t"
"v_mfma_f32_16x16x32_bf16 a[88:91], a[116:119], v[244:247], a[88:91]\n\t"
"v_mfma_f32_16x16x32_bf16 a[84:87], a[112:115], v[244:247], a[84:87]\n\t"
"v_mfma_f32_16x16x32_bf16 a[84:87], a[96:99], v[128:131], a[84:87]\n\t"
"s_waitcnt lgkmcnt(0)\n\t"
"v_mfma_f32_16x16x32_bf16 a[0:3], a[128:131], v[188:191], a[0:3]\n\t"
"s_add_u32 m0, s26, 12288\n\t"
"s_add_u32 s41, s25, 2654208\n\t"
"v_mfma_f32_16x16x32_bf16 a[0:3], a[144:147], v[192:195], a[0:3]\n\t"
"buffer_load_dwordx4 v4, s[12:15], s41 offen lds\n\t"
"v_mfma_f32_16x16x32_bf16 a[4:7], a[148:151], v[192:195], a[4:7]\n\t"
"v_mfma_f32_16x16x32_bf16 a[4:7], a[132:135], v[188:191], a[4:7]\n\t"
"v_mfma_f32_16x16x32_bf16 a[8:11], a[136:139], v[188:191], a[8:11]\n\t"
"v_mfma_f32_16x16x32_bf16 a[8:11], a[152:155], v[192:195], a[8:11]\n\t"
"buffer_load_dwordx4 v5, s[12:15], s41 offen offset:1024 lds\n\t"
"v_mfma_f32_16x16x32_bf16 v[64:67], a[156:159], v[192:195], v[64:67]\n\t"
"v_mfma_f32_16x16x32_bf16 v[64:67], a[140:143], v[188:191], v[64:67]\n\t"
"v_mfma_f32_16x16x32_bf16 v[68:71], a[140:143], v[204:207], v[68:71]\n\t"
"v_mfma_f32_16x16x32_bf16 v[68:71], a[156:159], v[212:215], v[68:71]\n\t"
"buffer_load_dwordx4 v6, s[12:15], s41 offen offset:2048 lds\n\t"
"v_mfma_f32_16x16x32_bf16 a[20:23], a[152:155], v[212:215], a[20:23]\n\t"
"v_mfma_f32_16x16x32_bf16 a[20:23], a[136:139], v[204:207], a[20:23]\n\t"
"v_mov_b32_dpp v248, v100 row_shr:15 row_mask:0xf bank_mask:0x8\n\t"
"v_mfma_f32_16x16x32_bf16 a[16:19], a[132:135], v[204:207], a[16:19]\n\t"
"v_mfma_f32_16x16x32_bf16 a[16:19], a[148:151], v[212:215], a[16:19]\n\t"
"buffer_load_dwordx4 v7, s[12:15], s41 offen offset:3072 lds\n\t"
"v_mov_b32_dpp v249, v101 row_shr:15 row_mask:0xf bank_mask:0x8\n\t"
"v_mfma_f32_16x16x32_bf16 a[12:15], a[144:147], v[212:215], a[12:15]\n\t"
"v_mfma_f32_16x16x32_bf16 a[12:15], a[128:131], v[204:207], a[12:15]\n\t"
"v_mov_b32_dpp v250, v102 row_shr:15 row_mask:0xf bank_mask:0x8\n\t"
"v_mfma_f32_16x16x32_bf16 a[24:27], a[128:131], v[44:47], a[24:27]\n\t"
"s_add_u32 m0, s26, 8192\n\t"
"s_add_u32 s42, s25, 3670016\n\t"
"v_mfma_f32_16x16x32_bf16 a[24:27], a[144:147], v[176:179], a[24:27]\n\t"
"buffer_load_dwordx4 v4, s[12:15], s42 offen lds\n\t"
"v_mfma_f32_16x16x32_bf16 a[28:31], a[148:151], v[176:179], a[28:31]\n\t"
"v_mfma_f32_16x16x32_bf16 a[28:31], a[132:135], v[44:47], a[28:31]\n\t"
"v_mov_b32_dpp v251, v103 row_shr:15 row_mask:0xf bank_mask:0x8\n\t"
"v_mfma_f32_16x16x32_bf16 a[32:35], a[136:139], v[44:47], a[32:35]\n\t"
"v_mfma_f32_16x16x32_bf16 a[32:35], a[152:155], v[176:179], a[32:35]\n\t"
"buffer_load_dwordx4 v5, s[12:15], s42 offen offset:1024 lds\n\t"
"v_mfma_f32_16x16x32_bf16 v[72:75], a[156:159], v[176:179], v[72:75]\n\t"
"v_mfma_f32_16x16x32_bf16 v[72:75], a[140:143], v[44:47], v[72:75]\n\t"
"v_mov_b32_dpp v248, v188 row_shl:1 row_mask:0xf bank_mask:0xf\n\t"
"v_mfma_f32_16x16x32_bf16 v[76:79], a[140:143], v[184:187], v[76:79]\n\t"
"v_mfma_f32_16x16x32_bf16 v[76:79], a[156:159], v[196:199], v[76:79]\n\t"
"buffer_load_dwordx4 v6, s[12:15], s42 offen offset:2048 lds\n\t"
"v_mfma_f32_16x16x32_bf16 a[44:47], a[152:155], v[196:199], a[44:47]\n\t"
"v_mov_b32_dpp v249, v189 row_shl:1 row_mask:0xf bank_mask:0xf\n\t"
"v_mfma_f32_16x16x32_bf16 a[44:47], a[136:139], v[184:187], a[44:47]\n\t"
"v_mov_b32_dpp v250, v190 row_shl:1 row_mask:0xf bank_mask:0xf\n\t"
"v_mfma_f32_16x16x32_bf16 a[40:43], a[132:135], v[184:187], a[40:43]\n\t"
"v_mov_b32_dpp v251, v191 row_shl:1 row_mask:0xf bank_mask:0xf\n\t"
"v_mfma_f32_16x16x32_bf16 a[40:43], a[148:151], v[196:199], a[40:43]\n\t"
"buffer_load_dwordx4 v7, s[12:15], s42 offen offset:3072 lds\n\t"
"v_mfma_f32_16x16x32_bf16 a[36:39], a[144:147], v[196:199], a[36:39]\n\t"
"v_mfma_f32_16x16x32_bf16 a[36:39], a[128:131], v[184:187], a[36:39]\n\t"
"v_mfma_f32_16x16x32_bf16 a[48:51], a[128:131], v[200:203], a[48:51]\n\t"
"s_add_u32 m0, s26, 24576\n\t"
"s_add_u32 s40, s25, 3702784\n\t"
"v_mfma_f32_16x16x32_bf16 a[48:51], a[144:147], v[208:211], a[48:51]\n\t"
"buffer_load_dwordx4 v4, s[12:15], s40 offen lds\n\t"
"v_mfma_f32_16x16x32_bf16 a[52:55], a[148:151], v[208:211], a[52:55]\n\t"
"s_waitcnt vmcnt(19)\n\t"
"ds_read_b64_tr_b16 a[160:161], v12 offset:28672\n\t"
"v_mfma_f32_16x16x32_bf16 a[52:55], a[132:135], v[200:203], a[52:55]\n\t"
"ds_read_b64_tr_b16 a[162:163], v12 offset:29696\n\t"
"v_mfma_f32_16x16x32_bf16 a[56:59], a[136:139], v[200:203], a[56:59]\n\t"
"ds_read_b64_tr_b16 a[164:165], v13 offset:28672\n\t"
"v_mfma_f32_16x16x32_bf16 a[56:59], a[152:155], v[208:211], a[56:59]\n\t"
"buffer_load_dwordx4 v5, s[12:15], s40 offen offset:1024 lds\n\t"
"v_mfma_f32_16x16x32_bf16 v[80:83], a[156:159], v[208:211], v[80:83]\n\t"
"ds_read_b64_tr_b16 a[166:167], v13 offset:29696\n\t"
"v_mfma_f32_16x16x32_bf16 v[80:83], a[140:143], v[200:203], v[80:83]\n\t"
"ds_read_b64_tr_b16 a[168:169], v14 offset:28672\n\t"
"v_mfma_f32_16x16x32_bf16 v[84:87], a[140:143], v[216:219], v[84:87]\n\t"
"ds_read_b64_tr_b16 a[170:171], v14 offset:29696\n\t"
"v_mfma_f32_16x16x32_bf16 v[84:87], a[156:159], v[224:227], v[84:87]\n\t"
"buffer_load_dwordx4 v6, s[12:15], s40 offen offset:2048 lds\n\t"
"v_mfma_f32_16x16x32_bf16 a[68:71], a[152:155], v[224:227], a[68:71]\n\t"
"ds_read_b64_tr_b16 a[172:173], v15 offset:28672\n\t"
"v_mfma_f32_16x16x32_bf16 a[68:71], a[136:139], v[216:219], a[68:71]\n\t"
"ds_read_b64_tr_b16 a[174:175], v15 offset:29696\n\t"
"v_mov_b32_dpp v252, v104 row_shr:15 row_mask:0xf bank_mask:0x8\n\t"
"v_mfma_f32_16x16x32_bf16 a[64:67], a[132:135], v[216:219], a[64:67]\n\t"
"s_waitcnt vmcnt(15)\n\t"
"ds_read_b64_tr_b16 a[176:177], v12 offset:0\n\t"
"v_mfma_f32_16x16x32_bf16 a[64:67], a[148:151], v[224:227], a[64:67]\n\t"
"buffer_load_dwordx4 v7, s[12:15], s40 offen offset:3072 lds\n\t"
"v_mov_b32_dpp v253, v105 row_shr:15 row_mask:0xf bank_mask:0x8\n\t"
"v_mfma_f32_16x16x32_bf16 a[60:63], a[144:147], v[224:227], a[60:63]\n\t"
"ds_read_b64_tr_b16 a[178:179], v12 offset:1024\n\t"
"v_mfma_f32_16x16x32_bf16 a[60:63], a[128:131], v[216:219], a[60:63]\n\t"
"v_mov_b32_dpp v254, v106 row_shr:15 row_mask:0xf bank_mask:0x8\n\t"
"v_mfma_f32_16x16x32_bf16 a[72:75], a[128:131], v[228:231], a[72:75]\n\t"
"s_add_u32 m0, s26, 16384\n\t"
"s_add_u32 s41, s25, 4718592\n\t"
"ds_read_b64_tr_b16 a[180:181], v13 offset:0\n\t"
"v_mfma_f32_16x16x32_bf16 a[72:75], a[144:147], v[232:235], a[72:75]\n\t"
"buffer_load_dwordx4 v4, s[12:15], s41 offen lds\n\t"
"v_mfma_f32_16x16x32_bf16 a[76:79], a[148:151], v[232:235], a[76:79]\n\t"
"ds_read_b64_tr_b16 a[182:183], v13 offset:1024\n\t"
"v_mfma_f32_16x16x32_bf16 a[76:79], a[132:135], v[228:231], a[76:79]\n\t"
"v_mov_b32_dpp v255, v107 row_shr:15 row_mask:0xf bank_mask:0x8\n\t"
"v_mfma_f32_16x16x32_bf16 a[80:83], a[136:139], v[228:231], a[80:83]\n\t"
"ds_read_b64_tr_b16 a[184:185], v14 offset:0\n\t"
"v_mfma_f32_16x16x32_bf16 a[80:83], a[152:155], v[232:235], a[80:83]\n\t"
"buffer_load_dwordx4 v5, s[12:15], s41 offen offset:1024 lds\n\t"
"v_mfma_f32_16x16x32_bf16 v[88:91], a[156:159], v[232:235], v[88:91]\n\t"
"ds_read_b64_tr_b16 a[186:187], v14 offset:1024\n\t"
"v_mfma_f32_16x16x32_bf16 v[88:91], a[140:143], v[228:231], v[88:91]\n\t"
"ds_read_b64_tr_b16 a[188:189], v15 offset:0\n\t"
"v_mov_b32_dpp v252, v192 row_shl:1 row_mask:0xf bank_mask:0xf\n\t"
"v_mfma_f32_16x16x32_bf16 v[92:95], a[140:143], v[236:239], v[92:95]\n\t"
"ds_read_b64_tr_b16 a[190:191], v15 offset:1024\n\t"
"v_mfma_f32_16x16x32_bf16 v[92:95], a[156:159], v[240:243], v[92:95]\n\t"
"buffer_load_dwordx4 v6, s[12:15], s41 offen offset:2048 lds\n\t"
"v_mfma_f32_16x16x32_bf16 a[92:95], a[152:155], v[240:243], a[92:95]\n\t"
"v_mov_b32_dpp v253, v193 row_shl:1 row_mask:0xf bank_mask:0xf\n\t"
"v_mfma_f32_16x16x32_bf16 a[92:95], a[136:139], v[236:239], a[92:95]\n\t"
"v_mov_b32_dpp v254, v194 row_shl:1 row_mask:0xf bank_mask:0xf\n\t"
"v_mfma_f32_16x16x32_bf16 a[88:91], a[132:135], v[236:239], a[88:91]\n\t"
"v_mov_b32_dpp v255, v195 row_shl:1 row_mask:0xf bank_mask:0xf\n\t"
"v_mfma_f32_16x16x32_bf16 a[88:91], a[148:151], v[240:243], a[88:91]\n\t"
"buffer_load_dwordx4 v7, s[12:15], s41 offen offset:3072 lds\n\t"
"v_mfma_f32_16x16x32_bf16 a[84:87], a[144:147], v[240:243], a[84:87]\n\t"
"v_mfma_f32_16x16x32_bf16 a[84:87], a[128:131], v[236:239], a[84:87]\n\t"
"s_xor_b32 s43, s33, 6815744\n\t"
"buffer_load_dword v24, v97, s[12:15], s43 offen\n\t"
"s_xor_b32 s43, s27, 1152\n\t"
"s_mov_b32 exec_hi, 3\n\t"
"buffer_load_dword v24, v23, s[8:11], s43 offen\n\t"
"s_mov_b32 exec_hi, -1\n\t"
"s_waitcnt lgkmcnt(0)\n\t"
"v_mfma_f32_16x16x32_bf16 a[0:3], a[160:163], v[204:207], a[0:3]\n\t"
"v_mfma_f32_16x16x32_bf16 a[0:3], a[176:179], v[212:215], a[0:3]\n\t"
"v_mfma_f32_16x16x32_bf16 a[4:7], a[180:183], v[212:215], a[4:7]\n\t"
"v_mfma_f32_16x16x32_bf16 a[4:7], a[164:167], v[204:207], a[4:7]\n\t"
"v_mfma_f32_16x16x32_bf16 a[8:11], a[168:171], v[204:207], a[8:11]\n\t"
"v_mfma_f32_16x16x32_bf16 a[8:11], a[184:187], v[212:215], a[8:11]\n\t"
"v_mfma_f32_16x16x32_bf16 v[64:67], a[188:191], v[212:215], v[64:67]\n\t"
"v_mfma_f32_16x16x32_bf16 v[64:67], a[172:175], v[204:207], v[64:67]\n\t"
"v_mfma_f32_16x16x32_bf16 v[68:71], a[172:175], v[44:47], v[68:71]\n\t"
"v_mfma_f32_16x16x32_bf16 v[68:71], a[188:191], v[176:179], v[68:71]\n\t"
"v_mfma_f32_16x16x32_bf16 a[20:23], a[184:187], v[176:179], a[20:23]\n\t"
"v_mfma_f32_16x16x32_bf16 a[20:23], a[168:171], v[44:47], a[20:23]\n\t"
"v_mov_b32_dpp v40, v100 row_shr:14 row_mask:0xf bank_mask:0x8\n\t"
"v_mfma_f32_16x16x32_bf16 a[16:19], a[164:167], v[44:47], a[16:19]\n\t"
"v_mfma_f32_16x16x32_bf16 a[16:19], a[180:183], v[176:179], a[16:19]\n\t"
"v_mov_b32_dpp v41, v101 row_shr:14 row_mask:0xf bank_mask:0x8\n\t"
"v_mfma_f32_16x16x32_bf16 a[12:15], a[176:179], v[176:179], a[12:15]\n\t"
"v_mfma_f32_16x16x32_bf16 a[12:15], a[160:163], v[44:47], a[12:15]\n\t"
"v_mov_b32_dpp v42, v102 row_shr:14 row_mask:0xf bank_mask:0x8\n\t"
"v_mfma_f32_16x16x32_bf16 a[24:27], a[160:163], v[184:187], a[24:27]\n\t"
"v_mfma_f32_16x16x32_bf16 a[24:27], a[176:179], v[196:199], a[24:27]\n\t"
"v_mfma_f32_16x16x32_bf16 a[28:31], a[180:183], v[196:199], a[28:31]\n\t"
"v_mfma_f32_16x16x32_bf16 a[28:31], a[164:167], v[184:187], a[28:31]\n\t"
"v_mov_b32_dpp v43, v103 row_shr:14 row_mask:0xf bank_mask:0x8\n\t"
"v_mfma_f32_16x16x32_bf16 a[32:35], a[168:171], v[184:187], a[32:35]\n\t"
"v_mfma_f32_16x16x32_bf16 a[32:35], a[184:187], v[196:199], a[32:35]\n\t"
"v_mfma_f32_16x16x32_bf16 v[72:75], a[188:191], v[196:199], v[72:75]\n\t"
"v_mfma_f32_16x16x32_bf16 v[72:75], a[172:175], v[184:187], v[72:75]\n\t"
"v_mov_b32_dpp v40, v204 row_shl:1 row_mask:0xf bank_mask:0xf\n\t"
"v_mfma_f32_16x16x32_bf16 v[76:79], a[172:175], v[200:203], v[76:79]\n\t"
"v_mfma_f32_16x16x32_bf16 v[76:79], a[188:191], v[208:211], v[76:79]\n\t"
"v_mfma_f32_16x16x32_bf16 a[44:47], a[184:187], v[208:211], a[44:47]\n\t"
"v_mov_b32_dpp v41, v205 row_shl:1 row_mask:0xf bank_mask:0xf\n\t"
"v_mfma_f32_16x16x32_bf16 a[44:47], a[168:171], v[200:203], a[44:47]\n\t"
"v_mov_b32_dpp v42, v206 row_shl:1 row_mask:0xf bank_mask:0xf\n\t"
"v_mfma_f32_16x16x32_bf16 a[40:43], a[164:167], v[200:203], a[40:43]\n\t"
"v_mov_b32_dpp v43, v207 row_shl:1 row_mask:0xf bank_mask:0xf\n\t"
"v_mfma_f32_16x16x32_bf16 a[40:43], a[180:183], v[208:211], a[40:43]\n\t"
"v_mfma_f32_16x16x32_bf16 a[36:39], a[176:179], v[208:211], a[36:39]\n\t"
"v_mfma_f32_16x16x32_bf16 a[36:39], a[160:163], v[200:203], a[36:39]\n\t"
"s_xor_b32 s43, s33, 589824\n\t"
"buffer_load_dword v24, v22, s[12:15], s43 offen\n\t"
"v_mfma_f32_16x16x32_bf16 a[48:51], a[160:163], v[216:219], a[48:51]\n\t"
"v_mfma_f32_16x16x32_bf16 a[48:51], a[176:179], v[224:227], a[48:51]\n\t"
"v_mfma_f32_16x16x32_bf16 a[52:55], a[180:183], v[224:227], a[52:55]\n\t"
"s_waitcnt vmcnt(19)\n\t"
"ds_read_b64_tr_b16 a[192:193], v12 offset:4096\n\t"
"v_mfma_f32_16x16x32_bf16 a[52:55], a[164:167], v[216:219], a[52:55]\n\t"
"ds_read_b64_tr_b16 a[194:195], v12 offset:5120\n\t"
"v_mfma_f32_16x16x32_bf16 a[56:59], a[168:171], v[216:219], a[56:59]\n\t"
"ds_read_b64_tr_b16 a[196:197], v13 offset:4096\n\t"
"v_mfma_f32_16x16x32_bf16 a[56:59], a[184:187], v[224:227], a[56:59]\n\t"
"v_mfma_f32_16x16x32_bf16 v[80:83], a[188:191], v[224:227], v[80:83]\n\t"
"ds_read_b64_tr_b16 a[198:199], v13 offset:5120\n\t"
"v_mfma_f32_16x16x32_bf16 v[80:83], a[172:175], v[216:219], v[80:83]\n\t"
"ds_read_b64_tr_b16 a[200:201], v14 offset:4096\n\t"
"v_mfma_f32_16x16x32_bf16 v[84:87], a[172:175], v[228:231], v[84:87]\n\t"
"ds_read_b64_tr_b16 a[202:203], v14 offset:5120\n\t"
"v_mfma_f32_16x16x32_bf16 v[84:87], a[188:191], v[232:235], v[84:87]\n\t"
"v_mfma_f32_16x16x32_bf16 a[68:71], a[184:187], v[232:235], a[68:71]\n\t"
"ds_read_b64_tr_b16 a[204:205], v15 offset:4096\n\t"
"v_mfma_f32_16x16x32_bf16 a[68:71], a[168:171], v[228:231], a[68:71]\n\t"
"ds_read_b64_tr_b16 a[206:207], v15 offset:5120\n\t"
"v_mov_b32_dpp v48, v104 row_shr:14 row_mask:0xf bank_mask:0x8\n\t"
"v_mfma_f32_16x16x32_bf16 a[64:67], a[164:167], v[228:231], a[64:67]\n\t"
"s_waitcnt vmcnt(15)\n\t"
"ds_read_b64_tr_b16 a[208:209], v12 offset:12288\n\t"
"v_mfma_f32_16x16x32_bf16 a[64:67], a[180:183], v[232:235], a[64:67]\n\t"
"v_mov_b32_dpp v49, v105 row_shr:14 row_mask:0xf bank_mask:0x8\n\t"
"v_mfma_f32_16x16x32_bf16 a[60:63], a[176:179], v[232:235], a[60:63]\n\t"
"ds_read_b64_tr_b16 a[210:211], v12 offset:13312\n\t"
"v_mfma_f32_16x16x32_bf16 a[60:63], a[160:163], v[228:231], a[60:63]\n\t"
"v_mov_b32_dpp v50, v106 row_shr:14 row_mask:0xf bank_mask:0x8\n\t"
"v_mfma_f32_16x16x32_bf16 a[72:75], a[160:163], v[236:239], a[72:75]\n\t"
"ds_read_b64_tr_b16 a[212:213], v13 offset:12288\n\t"
"v_mfma_f32_16x16x32_bf16 a[72:75], a[176:179], v[240:243], a[72:75]\n\t"
"v_mfma_f32_16x16x32_bf16 a[76:79], a[180:183], v[240:243], a[76:79]\n\t"
"ds_read_b64_tr_b16 a[214:215], v13 offset:13312\n\t"
"v_mfma_f32_16x16x32_bf16 a[76:79], a[164:167], v[236:239], a[76:79]\n\t"
"v_mov_b32_dpp v51, v107 row_shr:14 row_mask:0xf bank_mask:0x8\n\t"
"v_mfma_f32_16x16x32_bf16 a[80:83], a[168:171], v[236:239], a[80:83]\n\t"
"ds_read_b64_tr_b16 a[216:217], v14 offset:12288\n\t"
"v_mfma_f32_16x16x32_bf16 a[80:83], a[184:187], v[240:243], a[80:83]\n\t"
"v_mfma_f32_16x16x32_bf16 v[88:91], a[188:191], v[240:243], v[88:91]\n\t"
"ds_read_b64_tr_b16 a[218:219], v14 offset:13312\n\t"
"v_mfma_f32_16x16x32_bf16 v[88:91], a[172:175], v[236:239], v[88:91]\n\t"
"ds_read_b64_tr_b16 a[220:221], v15 offset:12288\n\t"
"v_mov_b32_dpp v48, v212 row_shl:1 row_mask:0xf bank_mask:0xf\n\t"
"v_mfma_f32_16x16x32_bf16 v[92:95], a[172:175], v[248:251], v[92:95]\n\t"
"ds_read_b64_tr_b16 a[222:223], v15 offset:13312\n\t"
"v_mfma_f32_16x16x32_bf16 v[92:95], a[188:191], v[252:255], v[92:95]\n\t"
"v_mfma_f32_16x16x32_bf16 a[92:95], a[184:187], v[252:255], a[92:95]\n\t"
"v_mov_b32_dpp v49, v213 row_shl:1 row_mask:0xf bank_mask:0xf\n\t"
"v_mfma_f32_16x16x32_bf16 a[92:95], a[168:171], v[248:251], a[92:95]\n\t"
"v_mov_b32_dpp v50, v214 row_shl:1 row_mask:0xf bank_mask:0xf\n\t"
"v_mfma_f32_16x16x32_bf16 a[88:91], a[164:167], v[248:251], a[88:91]\n\t"
"v_mov_b32_dpp v51, v215 row_shl:1 row_mask:0xf bank_mask:0xf\n\t"
"v_mfma_f32_16x16x32_bf16 a[88:91], a[180:183], v[252:255], a[88:91]\n\t"
"v_mfma_f32_16x16x32_bf16 a[84:87], a[176:179], v[252:255], a[84:87]\n\t"
"v_mfma_f32_16x16x32_bf16 a[84:87], a[160:163], v[248:251], a[84:87]\n\t"
"s_waitcnt lgkmcnt(0)\n\t"
"v_mfma_f32_16x16x32_bf16 a[0:3], a[192:195], v[44:47], a[0:3]\n\t"
"v_mfma_f32_16x16x32_bf16 a[0:3], a[208:211], v[176:179], a[0:3]\n\t"
"v_mfma_f32_16x16x32_bf16 a[4:7], a[212:215], v[176:179], a[4:7]\n\t"
"v_mfma_f32_16x16x32_bf16 a[4:7], a[196:199], v[44:47], a[4:7]\n\t"
"v_mfma_f32_16x16x32_bf16 a[8:11], a[200:203], v[44:47], a[8:11]\n\t"
"v_mfma_f32_16x16x32_bf16 a[8:11], a[216:219], v[176:179], a[8:11]\n\t"
"v_mfma_f32_16x16x32_bf16 v[64:67], a[220:223], v[176:179], v[64:67]\n\t"
"v_mfma_f32_16x16x32_bf16 v[64:67], a[204:207], v[44:47], v[64:67]\n\t"
"v_mfma_f32_16x16x32_bf16 v[68:71], a[204:207], v[184:187], v[68:71]\n\t"
"v_mfma_f32_16x16x32_bf16 v[68:71], a[220:223], v[196:199], v[68:71]\n\t"
"v_mfma_f32_16x16x32_bf16 a[20:23], a[216:219], v[196:199], a[20:23]\n\t"
"v_mfma_f32_16x16x32_bf16 a[20:23], a[200:203], v[184:187], a[20:23]\n\t"
"v_mov_b32_dpp v52, v100 row_shr:13 row_mask:0xf bank_mask:0x8\n\t"
"v_mfma_f32_16x16x32_bf16 a[16:19], a[196:199], v[184:187], a[16:19]\n\t"
"v_mfma_f32_16x16x32_bf16 a[16:19], a[212:215], v[196:199], a[16:19]\n\t"
"v_mov_b32_dpp v53, v101 row_shr:13 row_mask:0xf bank_mask:0x8\n\t"
"v_mfma_f32_16x16x32_bf16 a[12:15], a[208:211], v[196:199], a[12:15]\n\t"
"v_mfma_f32_16x16x32_bf16 a[12:15], a[192:195], v[184:187], a[12:15]\n\t"
"v_mov_b32_dpp v54, v102 row_shr:13 row_mask:0xf bank_mask:0x8\n\t"
"v_mfma_f32_16x16x32_bf16 a[24:27], a[192:195], v[200:203], a[24:27]\n\t"
"v_mfma_f32_16x16x32_bf16 a[24:27], a[208:211], v[208:211], a[24:27]\n\t"
"v_mfma_f32_16x16x32_bf16 a[28:31], a[212:215], v[208:211], a[28:31]\n\t"
"v_mfma_f32_16x16x32_bf16 a[28:31], a[196:199], v[200:203], a[28:31]\n\t"
"v_mov_b32_dpp v55, v103 row_shr:13 row_mask:0xf bank_mask:0x8\n\t"
"v_mfma_f32_16x16x32_bf16 a[32:35], a[200:203], v[200:203], a[32:35]\n\t"
"v_mfma_f32_16x16x32_bf16 a[32:35], a[216:219], v[208:211], a[32:35]\n\t"
"v_mfma_f32_16x16x32_bf16 v[72:75], a[220:223], v[208:211], v[72:75]\n\t"
"v_mfma_f32_16x16x32_bf16 v[72:75], a[204:207], v[200:203], v[72:75]\n\t"
"v_mov_b32_dpp v52, v44 row_shl:1 row_mask:0xf bank_mask:0xf\n\t"
"v_mfma_f32_16x16x32_bf16 v[76:79], a[204:207], v[216:219], v[76:79]\n\t"
"v_mfma_f32_16x16x32_bf16 v[76:79], a[220:223], v[224:227], v[76:79]\n\t"
"v_mfma_f32_16x16x32_bf16 a[44:47], a[216:219], v[224:227], a[44:47]\n\t"
"v_mov_b32_dpp v53, v45 row_shl:1 row_mask:0xf bank_mask:0xf\n\t"
"v_mfma_f32_16x16x32_bf16 a[44:47], a[200:203], v[216:219], a[44:47]\n\t"
"v_mov_b32_dpp v54, v46 row_shl:1 row_mask:0xf bank_mask:0xf\n\t"
"v_mfma_f32_16x16x32_bf16 a[40:43], a[196:199], v[216:219], a[40:43]\n\t"
"v_mov_b32_dpp v55, v47 row_shl:1 row_mask:0xf bank_mask:0xf\n\t"
"v_mfma_f32_16x16x32_bf16 a[40:43], a[212:215], v[224:227], a[40:43]\n\t"
"v_mfma_f32_16x16x32_bf16 a[36:39], a[208:211], v[224:227], a[36:39]\n\t"
"v_mfma_f32_16x16x32_bf16 a[36:39], a[192:195], v[216:219], a[36:39]\n\t"
"v_mfma_f32_16x16x32_bf16 a[48:51], a[192:195], v[228:231], a[48:51]\n\t"
"s_add_u32 m0, s26, 20480\n\t"
"s_add_u32 s42, s25, 4751360\n\t"
"v_mfma_f32_16x16x32_bf16 a[48:51], a[208:211], v[232:235], a[48:51]\n\t"
"buffer_load_dwordx4 v4, s[12:15], s42 offen lds\n\t"
"v_mfma_f32_16x16x32_bf16 a[52:55], a[212:215], v[232:235], a[52:55]\n\t"
"s_waitcnt vmcnt(12)\n\t"
"ds_read_b64_tr_b16 a[224:225], v12 offset:8192\n\t"
"v_mfma_f32_16x16x32_bf16 a[52:55], a[196:199], v[228:231], a[52:55]\n\t"
"ds_read_b64_tr_b16 a[226:227], v12 offset:9216\n\t"
"v_mfma_f32_16x16x32_bf16 a[56:59], a[200:203], v[228:231], a[56:59]\n\t"
"ds_read_b64_tr_b16 a[228:229], v13 offset:8192\n\t"
"v_mfma_f32_16x16x32_bf16 a[56:59], a[216:219], v[232:235], a[56:59]\n\t"
"v_mfma_f32_16x16x32_bf16 v[80:83], a[220:223], v[232:235], v[80:83]\n\t"
"ds_read_b64_tr_b16 a[230:231], v13 offset:9216\n\t"
"v_mfma_f32_16x16x32_bf16 v[80:83], a[204:207], v[228:231], v[80:83]\n\t"
"ds_read_b64_tr_b16 a[232:233], v14 offset:8192\n\t"
"v_mfma_f32_16x16x32_bf16 v[84:87], a[204:207], v[236:239], v[84:87]\n\t"
"ds_read_b64_tr_b16 a[234:235], v14 offset:9216\n\t"
"v_mfma_f32_16x16x32_bf16 v[84:87], a[220:223], v[240:243], v[84:87]\n\t"
"buffer_load_dwordx4 v5, s[12:15], s42 offen offset:1024 lds\n\t"
"v_mfma_f32_16x16x32_bf16 a[68:71], a[216:219], v[240:243], a[68:71]\n\t"
"ds_read_b64_tr_b16 a[236:237], v15 offset:8192\n\t"
"v_mfma_f32_16x16x32_bf16 a[68:71], a[200:203], v[236:239], a[68:71]\n\t"
"ds_read_b64_tr_b16 a[238:239], v15 offset:9216\n\t"
"v_mov_b32_dpp v56, v104 row_shr:13 row_mask:0xf bank_mask:0x8\n\t"
"v_mfma_f32_16x16x32_bf16 a[64:67], a[196:199], v[236:239], a[64:67]\n\t"
"s_waitcnt vmcnt(9)\n\t"
"ds_read_b64_tr_b16 a[240:241], v12 offset:24576\n\t"
"v_mfma_f32_16x16x32_bf16 a[64:67], a[212:215], v[240:243], a[64:67]\n\t"
"v_mov_b32_dpp v57, v105 row_shr:13 row_mask:0xf bank_mask:0x8\n\t"
"v_mfma_f32_16x16x32_bf16 a[60:63], a[208:211], v[240:243], a[60:63]\n\t"
"ds_read_b64_tr_b16 a[242:243], v12 offset:25600\n\t"
"v_mfma_f32_16x16x32_bf16 a[60:63], a[192:195], v[236:239], a[60:63]\n\t"
"v_mov_b32_dpp v58, v106 row_shr:13 row_mask:0xf bank_mask:0x8\n\t"
"v_mfma_f32_16x16x32_bf16 a[72:75], a[192:195], v[248:251], a[72:75]\n\t"
"ds_read_b64_tr_b16 a[244:245], v13 offset:24576\n\t"
"v_mfma_f32_16x16x32_bf16 a[72:75], a[208:211], v[252:255], a[72:75]\n\t"
"buffer_load_dwordx4 v6, s[12:15], s42 offen offset:2048 lds\n\t"
"v_mfma_f32_16x16x32_bf16 a[76:79], a[212:215], v[252:255], a[76:79]\n\t"
"ds_read_b64_tr_b16 a[246:247], v13 offset:25600\n\t"
"v_mfma_f32_16x16x32_bf16 a[76:79], a[196:199], v[248:251], a[76:79]\n\t"
"v_mov_b32_dpp v59, v107 row_shr:13 row_mask:0xf bank_mask:0x8\n\t"
"v_mfma_f32_16x16x32_bf16 a[80:83], a[200:203], v[248:251], a[80:83]\n\t"
"ds_read_b64_tr_b16 a[248:249], v14 offset:24576\n\t"
"v_mfma_f32_16x16x32_bf16 a[80:83], a[216:219], v[252:255], a[80:83]\n\t"
"v_mfma_f32_16x16x32_bf16 v[88:91], a[220:223], v[252:255], v[88:91]\n\t"
"ds_read_b64_tr_b16 a[250:251], v14 offset:25600\n\t"
"v_mfma_f32_16x16x32_bf16 v[88:91], a[204:207], v[248:251], v[88:91]\n\t"
"ds_read_b64_tr_b16 a[252:253], v15 offset:24576\n\t"
"v_mov_b32_dpp v56, v176 row_shl:1 row_mask:0xf bank_mask:0xf\n\t"
"v_mfma_f32_16x16x32_bf16 v[92:95], a[204:207], v[40:43], v[92:95]\n\t"
"ds_read_b64_tr_b16 a[254:255], v15 offset:25600\n\t"
"v_mfma_f32_16x16x32_bf16 v[92:95], a[220:223], v[48:51], v[92:95]\n\t"
"buffer_load_dwordx4 v7, s[12:15], s42 offen offset:3072 lds\n\t"
"v_mfma_f32_16x16x32_bf16 a[92:95], a[216:219], v[48:51], a[92:95]\n\t"
"v_mov_b32_dpp v57, v177 row_shl:1 row_mask:0xf bank_mask:0xf\n\t"
"v_mfma_f32_16x16x32_bf16 a[92:95], a[200:203], v[40:43], a[92:95]\n\t"
"v_mov_b32_dpp v58, v178 row_shl:1 row_mask:0xf bank_mask:0xf\n\t"
"v_mfma_f32_16x16x32_bf16 a[88:91], a[196:199], v[40:43], a[88:91]\n\t"
"v_mov_b32_dpp v59, v179 row_shl:1 row_mask:0xf bank_mask:0xf\n\t"
"v_mfma_f32_16x16x32_bf16 a[88:91], a[212:215], v[48:51], a[88:91]\n\t"
"v_mfma_f32_16x16x32_bf16 a[84:87], a[208:211], v[48:51], a[84:87]\n\t"
"v_mfma_f32_16x16x32_bf16 a[84:87], a[192:195], v[40:43], a[84:87]\n\t"
"s_waitcnt lgkmcnt(0)\n\t"
"v_mfma_f32_16x16x32_bf16 a[0:3], a[224:227], v[184:187], a[0:3]\n\t"
"s_add_u32 m0, s26, 32768\n\t"
"s_add_u32 s40, s25, 5767168\n\t"
"v_mfma_f32_16x16x32_bf16 a[0:3], a[240:243], v[196:199], a[0:3]\n\t"
"buffer_load_dwordx4 v4, s[12:15], s40 offen lds\n\t"
"v_mfma_f32_16x16x32_bf16 a[4:7], a[244:247], v[196:199], a[4:7]\n\t"
"v_mfma_f32_16x16x32_bf16 a[4:7], a[228:231], v[184:187], a[4:7]\n\t"
"v_mfma_f32_16x16x32_bf16 a[8:11], a[232:235], v[184:187], a[8:11]\n\t"
"v_mfma_f32_16x16x32_bf16 a[8:11], a[248:251], v[196:199], a[8:11]\n\t"
"buffer_load_dwordx4 v5, s[12:15], s40 offen offset:1024 lds\n\t"
"v_mfma_f32_16x16x32_bf16 v[64:67], a[252:255], v[196:199], v[64:67]\n\t"
"v_mfma_f32_16x16x32_bf16 v[64:67], a[236:239], v[184:187], v[64:67]\n\t"
"v_mfma_f32_16x16x32_bf16 v[68:71], a[236:239], v[200:203], v[68:71]\n\t"
"v_mfma_f32_16x16x32_bf16 v[68:71], a[252:255], v[208:211], v[68:71]\n\t"
"buffer_load_dwordx4 v6, s[12:15], s40 offen offset:2048 lds\n\t"
"v_mfma_f32_16x16x32_bf16 a[20:23], a[248:251], v[208:211], a[20:23]\n\t"
"v_mfma_f32_16x16x32_bf16 a[20:23], a[232:235], v[200:203], a[20:23]\n\t"
"v_mov_b32_dpp v60, v100 row_shr:12 row_mask:0xf bank_mask:0x8\n\t"
"v_mfma_f32_16x16x32_bf16 a[16:19], a[228:231], v[200:203], a[16:19]\n\t"
"v_mfma_f32_16x16x32_bf16 a[16:19], a[244:247], v[208:211], a[16:19]\n\t"
"buffer_load_dwordx4 v7, s[12:15], s40 offen offset:3072 lds\n\t"
"v_mov_b32_dpp v61, v101 row_shr:12 row_mask:0xf bank_mask:0x8\n\t"
"v_mfma_f32_16x16x32_bf16 a[12:15], a[240:243], v[208:211], a[12:15]\n\t"
"v_mfma_f32_16x16x32_bf16 a[12:15], a[224:227], v[200:203], a[12:15]\n\t"
"v_mov_b32_dpp v62, v102 row_shr:12 row_mask:0xf bank_mask:0x8\n\t"
"v_mfma_f32_16x16x32_bf16 a[24:27], a[224:227], v[216:219], a[24:27]\n\t"
"s_add_u32 m0, s26, 36864\n\t"
"s_add_u32 s41, s25, 5799936\n\t"
"v_mfma_f32_16x16x32_bf16 a[24:27], a[240:243], v[224:227], a[24:27]\n\t"
"buffer_load_dwordx4 v4, s[12:15], s41 offen lds\n\t"
"v_mfma_f32_16x16x32_bf16 a[28:31], a[244:247], v[224:227], a[28:31]\n\t"
"v_mfma_f32_16x16x32_bf16 a[28:31], a[228:231], v[216:219], a[28:31]\n\t"
"v_mov_b32_dpp v63, v103 row_shr:12 row_mask:0xf bank_mask:0x8\n\t"
"v_mfma_f32_16x16x32_bf16 a[32:35], a[232:235], v[216:219], a[32:35]\n\t"
"v_mfma_f32_16x16x32_bf16 a[32:35], a[248:251], v[224:227], a[32:35]\n\t"
"buffer_load_dwordx4 v5, s[12:15], s41 offen offset:1024 lds\n\t"
"v_mfma_f32_16x16x32_bf16 v[72:75], a[252:255], v[224:227], v[72:75]\n\t"
"v_mfma_f32_16x16x32_bf16 v[72:75], a[236:239], v[216:219], v[72:75]\n\t"
"v_mov_b32_dpp v60, v184 row_shl:1 row_mask:0xf bank_mask:0xf\n\t"
"v_mfma_f32_16x16x32_bf16 v[76:79], a[236:239], v[228:231], v[76:79]\n\t"
"v_mfma_f32_16x16x32_bf16 v[76:79], a[252:255], v[232:235], v[76:79]\n\t"
"buffer_load_dwordx4 v6, s[12:15], s41 offen offset:2048 lds\n\t"
"v_mfma_f32_16x16x32_bf16 a[44:47], a[248:251], v[232:235], a[44:47]\n\t"
"v_mov_b32_dpp v61, v185 row_shl:1 row_mask:0xf bank_mask:0xf\n\t"
"v_mfma_f32_16x16x32_bf16 a[44:47], a[232:235], v[228:231], a[44:47]\n\t"
"v_mov_b32_dpp v62, v186 row_shl:1 row_mask:0xf bank_mask:0xf\n\t"
"v_mfma_f32_16x16x32_bf16 a[40:43], a[228:231], v[228:231], a[40:43]\n\t"
"v_mov_b32_dpp v63, v187 row_shl:1 row_mask:0xf bank_mask:0xf\n\t"
"v_mfma_f32_16x16x32_bf16 a[40:43], a[244:247], v[232:235], a[40:43]\n\t"
"buffer_load_dwordx4 v7, s[12:15], s41 offen offset:3072 lds\n\t"
"v_mfma_f32_16x16x32_bf16 a[36:39], a[240:243], v[232:235], a[36:39]\n\t"
"v_mfma_f32_16x16x32_bf16 a[36:39], a[224:227], v[228:231], a[36:39]\n\t"
"v_mfma_f32_16x16x32_bf16 a[48:51], a[224:227], v[236:239], a[48:51]\n\t"
"s_add_u32 m0, s26, 28672\n\t"
"s_add_u32 s42, s7, 1024\n\t"
"v_mfma_f32_16x16x32_bf16 a[48:51], a[240:243], v[240:243], a[48:51]\n\t"
"buffer_load_dwordx4 v8, s[8:11], s42 offen lds\n\t"
"v_mfma_f32_16x16x32_bf16 a[52:55], a[244:247], v[240:243], a[52:55]\n\t"
"s_waitcnt vmcnt(16)\n\t"
"ds_read_b64_tr_b16 a[96:97], v12 offset:16384\n\t"
"v_mfma_f32_16x16x32_bf16 a[52:55], a[228:231], v[236:239], a[52:55]\n\t"
"ds_read_b64_tr_b16 a[98:99], v12 offset:17408\n\t"
"v_mfma_f32_16x16x32_bf16 a[56:59], a[232:235], v[236:239], a[56:59]\n\t"
"ds_read_b64_tr_b16 a[100:101], v13 offset:16384\n\t"
"v_mfma_f32_16x16x32_bf16 a[56:59], a[248:251], v[240:243], a[56:59]\n\t"
"buffer_load_dwordx4 v9, s[8:11], s42 offen offset:1024 lds\n\t"
"v_mfma_f32_16x16x32_bf16 v[80:83], a[252:255], v[240:243], v[80:83]\n\t"
"ds_read_b64_tr_b16 a[102:103], v13 offset:17408\n\t"
"v_mfma_f32_16x16x32_bf16 v[80:83], a[236:239], v[236:239], v[80:83]\n\t"
"ds_read_b64_tr_b16 a[104:105], v14 offset:16384\n\t"
"v_mfma_f32_16x16x32_bf16 v[84:87], a[236:239], v[248:251], v[84:87]\n\t"
"ds_read_b64_tr_b16 a[106:107], v14 offset:17408\n\t"
"v_mfma_f32_16x16x32_bf16 v[84:87], a[252:255], v[252:255], v[84:87]\n\t"
"buffer_load_dwordx4 v8, s[8:11], s42 offen offset:2048 lds\n\t"
"v_mfma_f32_16x16x32_bf16 a[68:71], a[248:251], v[252:255], a[68:71]\n\t"
"ds_read_b64_tr_b16 a[108:109], v15 offset:16384\n\t"
"v_mfma_f32_16x16x32_bf16 a[68:71], a[232:235], v[248:251], a[68:71]\n\t"
"ds_read_b64_tr_b16 a[110:111], v15 offset:17408\n\t"
"v_mov_b32_dpp v116, v104 row_shr:12 row_mask:0xf bank_mask:0x8\n\t"
"v_mfma_f32_16x16x32_bf16 a[64:67], a[228:231], v[248:251], a[64:67]\n\t"
"s_waitcnt vmcnt(11)\n\t"
"ds_read_b64_tr_b16 a[112:113], v12 offset:20480\n\t"
"v_mfma_f32_16x16x32_bf16 a[64:67], a[244:247], v[252:255], a[64:67]\n\t"
"buffer_load_dwordx4 v9, s[8:11], s42 offen offset:3072 lds\n\t"
"v_mov_b32_dpp v117, v105 row_shr:12 row_mask:0xf bank_mask:0x8\n\t"
"v_mfma_f32_16x16x32_bf16 a[60:63], a[240:243], v[252:255], a[60:63]\n\t"
"ds_read_b64_tr_b16 a[114:115], v12 offset:21504\n\t"
"v_mfma_f32_16x16x32_bf16 a[60:63], a[224:227], v[248:251], a[60:63]\n\t"
"v_mov_b32_dpp v118, v106 row_shr:12 row_mask:0xf bank_mask:0x8\n\t"
"v_mfma_f32_16x16x32_bf16 a[72:75], a[224:227], v[40:43], a[72:75]\n\t"
"s_add_u32 m0, s26, 0\n\t"
"s_add_u32 s40, s25, 6815744\n\t"
"ds_read_b64_tr_b16 a[116:117], v13 offset:20480\n\t"
"v_mfma_f32_16x16x32_bf16 a[72:75], a[240:243], v[48:51], a[72:75]\n\t"
"buffer_load_dwordx4 v4, s[12:15], s40 offen lds\n\t"
"v_mfma_f32_16x16x32_bf16 a[76:79], a[244:247], v[48:51], a[76:79]\n\t"
"ds_read_b64_tr_b16 a[118:119], v13 offset:21504\n\t"
"v_mfma_f32_16x16x32_bf16 a[76:79], a[228:231], v[40:43], a[76:79]\n\t"
"v_mov_b32_dpp v119, v107 row_shr:12 row_mask:0xf bank_mask:0x8\n\t"
"v_mfma_f32_16x16x32_bf16 a[80:83], a[232:235], v[40:43], a[80:83]\n\t"
"ds_read_b64_tr_b16 a[120:121], v14 offset:20480\n\t"
"v_mfma_f32_16x16x32_bf16 a[80:83], a[248:251], v[48:51], a[80:83]\n\t"
"buffer_load_dwordx4 v5, s[12:15], s40 offen offset:1024 lds\n\t"
"v_mfma_f32_16x16x32_bf16 v[88:91], a[252:255], v[48:51], v[88:91]\n\t"
"ds_read_b64_tr_b16 a[122:123], v14 offset:21504\n\t"
"v_mfma_f32_16x16x32_bf16 v[88:91], a[236:239], v[40:43], v[88:91]\n\t"
"ds_read_b64_tr_b16 a[124:125], v15 offset:20480\n\t"
"v_mov_b32_dpp v116, v196 row_shl:1 row_mask:0xf bank_mask:0xf\n\t"
"v_mfma_f32_16x16x32_bf16 v[92:95], a[236:239], v[52:55], v[92:95]\n\t"
"ds_read_b64_tr_b16 a[126:127], v15 offset:21504\n\t"
"v_mfma_f32_16x16x32_bf16 v[92:95], a[252:255], v[56:59], v[92:95]\n\t"
"buffer_load_dwordx4 v6, s[12:15], s40 offen offset:2048 lds\n\t"
"v_mfma_f32_16x16x32_bf16 a[92:95], a[248:251], v[56:59], a[92:95]\n\t"
"v_mov_b32_dpp v117, v197 row_shl:1 row_mask:0xf bank_mask:0xf\n\t"
"v_mfma_f32_16x16x32_bf16 a[92:95], a[232:235], v[52:55], a[92:95]\n\t"
"v_mov_b32_dpp v118, v198 row_shl:1 row_mask:0xf bank_mask:0xf\n\t"
"v_mfma_f32_16x16x32_bf16 a[88:91], a[228:231], v[52:55], a[88:91]\n\t"
"v_mov_b32_dpp v119, v199 row_shl:1 row_mask:0xf bank_mask:0xf\n\t"
"v_mfma_f32_16x16x32_bf16 a[88:91], a[244:247], v[56:59], a[88:91]\n\t"
"buffer_load_dwordx4 v7, s[12:15], s40 offen offset:3072 lds\n\t"
"v_mfma_f32_16x16x32_bf16 a[84:87], a[240:243], v[56:59], a[84:87]\n\t"
"v_mfma_f32_16x16x32_bf16 a[84:87], a[224:227], v[52:55], a[84:87]\n\t"
"s_waitcnt lgkmcnt(0)\n\t"
"v_mfma_f32_16x16x32_bf16 a[0:3], a[96:99], v[200:203], a[0:3]\n\t"
"s_add_u32 m0, s26, 4096\n\t"
"s_add_u32 s41, s7, 5120\n\t"
"v_mfma_f32_16x16x32_bf16 a[0:3], a[112:115], v[208:211], a[0:3]\n\t"
"buffer_load_dwordx4 v8, s[8:11], s41 offen lds\n\t"
"v_mfma_f32_16x16x32_bf16 a[4:7], a[116:119], v[208:211], a[4:7]\n\t"
"v_mfma_f32_16x16x32_bf16 a[4:7], a[100:103], v[200:203], a[4:7]\n\t"
"v_mfma_f32_16x16x32_bf16 a[8:11], a[104:107], v[200:203], a[8:11]\n\t"
"v_mfma_f32_16x16x32_bf16 a[8:11], a[120:123], v[208:211], a[8:11]\n\t"
"buffer_load_dwordx4 v9, s[8:11], s41 offen offset:1024 lds\n\t"
"v_mfma_f32_16x16x32_bf16 v[64:67], a[124:127], v[208:211], v[64:67]\n\t"
"v_mfma_f32_16x16x32_bf16 v[64:67], a[108:111], v[200:203], v[64:67]\n\t"
"v_mfma_f32_16x16x32_bf16 v[68:71], a[108:111], v[216:219], v[68:71]\n\t"
"v_mfma_f32_16x16x32_bf16 v[68:71], a[124:127], v[224:227], v[68:71]\n\t"
"buffer_load_dwordx4 v8, s[8:11], s41 offen offset:2048 lds\n\t"
"v_mfma_f32_16x16x32_bf16 a[20:23], a[120:123], v[224:227], a[20:23]\n\t"
"v_mfma_f32_16x16x32_bf16 a[20:23], a[104:107], v[216:219], a[20:23]\n\t"
"v_mov_b32_dpp v120, v100 row_shr:11 row_mask:0xf bank_mask:0x8\n\t"
"v_mfma_f32_16x16x32_bf16 a[16:19], a[100:103], v[216:219], a[16:19]\n\t"
"v_mfma_f32_16x16x32_bf16 a[16:19], a[116:119], v[224:227], a[16:19]\n\t"
"buffer_load_dwordx4 v9, s[8:11], s41 offen offset:3072 lds\n\t"
"v_mov_b32_dpp v121, v101 row_shr:11 row_mask:0xf bank_mask:0x8\n\t"
"v_mfma_f32_16x16x32_bf16 a[12:15], a[112:115], v[224:227], a[12:15]\n\t"
"v_mfma_f32_16x16x32_bf16 a[12:15], a[96:99], v[216:219], a[12:15]\n\t"
"v_mov_b32_dpp v122, v102 row_shr:11 row_mask:0xf bank_mask:0x8\n\t"
"v_mfma_f32_16x16x32_bf16 a[24:27], a[96:99], v[228:231], a[24:27]\n\t"
"s_add_u32 m0, s26, 12288\n\t"
"s_add_u32 s42, s7, 9216\n\t"
"v_mfma_f32_16x16x32_bf16 a[24:27], a[112:115], v[232:235], a[24:27]\n\t"
"buffer_load_dwordx4 v8, s[8:11], s42 offen lds\n\t"
"v_mfma_f32_16x16x32_bf16 a[28:31], a[116:119], v[232:235], a[28:31]\n\t"
"v_mfma_f32_16x16x32_bf16 a[28:31], a[100:103], v[228:231], a[28:31]\n\t"
"v_mov_b32_dpp v123, v103 row_shr:11 row_mask:0xf bank_mask:0x8\n\t"
"v_mfma_f32_16x16x32_bf16 a[32:35], a[104:107], v[228:231], a[32:35]\n\t"
"v_mfma_f32_16x16x32_bf16 a[32:35], a[120:123], v[232:235], a[32:35]\n\t"
"buffer_load_dwordx4 v9, s[8:11], s42 offen offset:1024 lds\n\t"
"v_mfma_f32_16x16x32_bf16 v[72:75], a[124:127], v[232:235], v[72:75]\n\t"
"v_mfma_f32_16x16x32_bf16 v[72:75], a[108:111], v[228:231], v[72:75]\n\t"
"v_mov_b32_dpp v120, v200 row_shl:1 row_mask:0xf bank_mask:0xf\n\t"
"v_mfma_f32_16x16x32_bf16 v[76:79], a[108:111], v[236:239], v[76:79]\n\t"
"v_mfma_f32_16x16x32_bf16 v[76:79], a[124:127], v[240:243], v[76:79]\n\t"
"buffer_load_dwordx4 v8, s[8:11], s42 offen offset:2048 lds\n\t"
"v_mfma_f32_16x16x32_bf16 a[44:47], a[120:123], v[240:243], a[44:47]\n\t"
"v_mov_b32_dpp v121, v201 row_shl:1 row_mask:0xf bank_mask:0xf\n\t"
"v_mfma_f32_16x16x32_bf16 a[44:47], a[104:107], v[236:239], a[44:47]\n\t"
"v_mov_b32_dpp v122, v202 row_shl:1 row_mask:0xf bank_mask:0xf\n\t"
"v_mfma_f32_16x16x32_bf16 a[40:43], a[100:103], v[236:239], a[40:43]\n\t"
"v_mov_b32_dpp v123, v203 row_shl:1 row_mask:0xf bank_mask:0xf\n\t"
"v_mfma_f32_16x16x32_bf16 a[40:43], a[116:119], v[240:243], a[40:43]\n\t"
"buffer_load_dwordx4 v9, s[8:11], s42 offen offset:3072 lds\n\t"
"v_mfma_f32_16x16x32_bf16 a[36:39], a[112:115], v[240:243], a[36:39]\n\t"
"v_mfma_f32_16x16x32_bf16 a[36:39], a[96:99], v[236:239], a[36:39]\n\t"
"v_mfma_f32_16x16x32_bf16 a[48:51], a[96:99], v[248:251], a[48:51]\n\t"
"s_add_u32 m0, s26, 8192\n\t"
"s_add_u32 s40, s25, 6848512\n\t"
"v_mfma_f32_16x16x32_bf16 a[48:51], a[112:115], v[252:255], a[48:51]\n\t"
"buffer_load_dwordx4 v4, s[12:15], s40 offen lds\n\t"
"v_mfma_f32_16x16x32_bf16 a[52:55], a[116:119], v[252:255], a[52:55]\n\t"
"s_waitcnt vmcnt(21)\n\t"
"ds_read_b64_tr_b16 a[128:129], v12 offset:32768\n\t"
"s_waitcnt vmcnt(13)\n\t"
"ds_read_b128 v[124:127], v16 offset:28672\n\t"
"v_mfma_f32_16x16x32_bf16 a[52:55], a[100:103], v[248:251], a[52:55]\n\t"
"ds_read_b64_tr_b16 a[130:131], v12 offset:33792\n\t"
"v_mfma_f32_16x16x32_bf16 a[56:59], a[104:107], v[248:251], a[56:59]\n\t"
"ds_read_b64_tr_b16 a[132:133], v13 offset:32768\n\t"
"ds_read_b128 v[152:155], v17 offset:28672\n\t"
"v_mfma_f32_16x16x32_bf16 a[56:59], a[120:123], v[252:255], a[56:59]\n\t"
"buffer_load_dwordx4 v5, s[12:15], s40 offen offset:1024 lds\n\t"
"v_mfma_f32_16x16x32_bf16 v[80:83], a[124:127], v[252:255], v[80:83]\n\t"
"ds_read_b64_tr_b16 a[134:135], v13 offset:33792\n\t"
"ds_read_b128 v[160:163], v16 offset:30720\n\t"
"v_mfma_f32_16x16x32_bf16 v[80:83], a[108:111], v[248:251], v[80:83]\n\t"
"ds_read_b64_tr_b16 a[136:137], v14 offset:32768\n\t"
"v_mfma_f32_16x16x32_bf16 v[84:87], a[108:111], v[40:43], v[84:87]\n\t"
"ds_read_b64_tr_b16 a[138:139], v14 offset:33792\n\t"
"ds_read_b128 v[136:139], v17 offset:30720\n\t"
"v_mfma_f32_16x16x32_bf16 v[84:87], a[124:127], v[48:51], v[84:87]\n\t"
"buffer_load_dwordx4 v6, s[12:15], s40 offen offset:2048 lds\n\t"
"v_mfma_f32_16x16x32_bf16 a[68:71], a[120:123], v[48:51], a[68:71]\n\t"
"ds_read_b64_tr_b16 a[140:141], v15 offset:32768\n\t"
"v_mfma_f32_16x16x32_bf16 a[68:71], a[104:107], v[40:43], a[68:71]\n\t"
"ds_read_b64_tr_b16 a[142:143], v15 offset:33792\n\t"
"v_mov_b32_dpp v144, v104 row_shr:11 row_mask:0xf bank_mask:0x8\n\t"
"v_mfma_f32_16x16x32_bf16 a[64:67], a[100:103], v[40:43], a[64:67]\n\t"
"ds_read_b64_tr_b16 a[144:145], v12 offset:36864\n\t"
"v_mfma_f32_16x16x32_bf16 a[64:67], a[116:119], v[48:51], a[64:67]\n\t"
"buffer_load_dwordx4 v7, s[12:15], s40 offen offset:3072 lds\n\t"
"v_mov_b32_dpp v145, v105 row_shr:11 row_mask:0xf bank_mask:0x8\n\t"
"v_mfma_f32_16x16x32_bf16 a[60:63], a[112:115], v[48:51], a[60:63]\n\t"
"ds_read_b64_tr_b16 a[146:147], v12 offset:37888\n\t"
"v_mfma_f32_16x16x32_bf16 a[60:63], a[96:99], v[40:43], a[60:63]\n\t"
"v_mov_b32_dpp v146, v106 row_shr:11 row_mask:0xf bank_mask:0x8\n\t"
"v_mfma_f32_16x16x32_bf16 a[72:75], a[96:99], v[52:55], a[72:75]\n\t"
"s_add_u32 m0, s26, 24576\n\t"
"s_add_u32 s41, s6, 524288\n\t"
"ds_read_b64_tr_b16 a[148:149], v13 offset:36864\n\t"
"v_mfma_f32_16x16x32_bf16 a[72:75], a[112:115], v[56:59], a[72:75]\n\t"
"buffer_load_dwordx4 v4, s[12:15], s41 offen lds\n\t"
"v_mfma_f32_16x16x32_bf16 a[76:79], a[116:119], v[56:59], a[76:79]\n\t"
"ds_read_b64_tr_b16 a[150:151], v13 offset:37888\n\t"
"v_mfma_f32_16x16x32_bf16 a[76:79], a[100:103], v[52:55], a[76:79]\n\t"
"v_mov_b32_dpp v147, v107 row_shr:11 row_mask:0xf bank_mask:0x8\n\t"
"v_mfma_f32_16x16x32_bf16 a[80:83], a[104:107], v[52:55], a[80:83]\n\t"
"ds_read_b64_tr_b16 a[152:153], v14 offset:36864\n\t"
"v_mfma_f32_16x16x32_bf16 a[80:83], a[120:123], v[56:59], a[80:83]\n\t"
"buffer_load_dwordx4 v5, s[12:15], s41 offen offset:1024 lds\n\t"
"v_mfma_f32_16x16x32_bf16 v[88:91], a[124:127], v[56:59], v[88:91]\n\t"
"ds_read_b64_tr_b16 a[154:155], v14 offset:37888\n\t"
"v_mfma_f32_16x16x32_bf16 v[88:91], a[108:111], v[52:55], v[88:91]\n\t"
"ds_read_b64_tr_b16 a[156:157], v15 offset:36864\n\t"
"v_mov_b32_dpp v144, v208 row_shl:1 row_mask:0xf bank_mask:0xf\n\t"
"v_mfma_f32_16x16x32_bf16 v[92:95], a[108:111], v[60:63], v[92:95]\n\t"
"ds_read_b64_tr_b16 a[158:159], v15 offset:37888\n\t"
"v_mfma_f32_16x16x32_bf16 v[92:95], a[124:127], v[116:119], v[92:95]\n\t"
"buffer_load_dwordx4 v6, s[12:15], s41 offen offset:2048 lds\n\t"
"v_mfma_f32_16x16x32_bf16 a[92:95], a[120:123], v[116:119], a[92:95]\n\t"
"v_mov_b32_dpp v145, v209 row_shl:1 row_mask:0xf bank_mask:0xf\n\t"
"v_mfma_f32_16x16x32_bf16 a[92:95], a[104:107], v[60:63], a[92:95]\n\t"
"v_mov_b32_dpp v146, v210 row_shl:1 row_mask:0xf bank_mask:0xf\n\t"
"v_mfma_f32_16x16x32_bf16 a[88:91], a[100:103], v[60:63], a[88:91]\n\t"
"v_mov_b32_dpp v147, v211 row_shl:1 row_mask:0xf bank_mask:0xf\n\t"
"v_mfma_f32_16x16x32_bf16 a[88:91], a[116:119], v[116:119], a[88:91]\n\t"
"buffer_load_dwordx4 v7, s[12:15], s41 offen offset:3072 lds\n\t"
"v_mfma_f32_16x16x32_bf16 a[84:87], a[112:115], v[116:119], a[84:87]\n\t"
"v_mfma_f32_16x16x32_bf16 a[84:87], a[96:99], v[60:63], a[84:87]\n\t"
"s_waitcnt lgkmcnt(0)\n\t"
"s_waitcnt vmcnt(12)\n\t"
"ds_read_b128 v[168:171], v16 offset:4096\n\t"
"v_mfma_f32_16x16x32_bf16 a[0:3], a[128:131], v[216:219], a[0:3]\n\t"
"s_add_u32 m0, s26, 16384\n\t"
"s_add_u32 s42, s7, 13312\n\t"
"v_mfma_f32_16x16x32_bf16 a[0:3], a[144:147], v[224:227], a[0:3]\n\t"
"buffer_load_dwordx4 v8, s[8:11], s42 offen lds\n\t"
"v_mfma_f32_16x16x32_bf16 a[4:7], a[148:151], v[224:227], a[4:7]\n\t"
"s_waitcnt vmcnt(9)\n\t"
"ds_read_b128 v[156:159], v16 offset:12288\n\t"
"v_mfma_f32_16x16x32_bf16 a[4:7], a[132:135], v[216:219], a[4:7]\n\t"
"ds_read_b128 v[180:183], v17 offset:4096\n\t"
"v_mfma_f32_16x16x32_bf16 a[8:11], a[136:139], v[216:219], a[8:11]\n\t"
"ds_read_b128 v[132:135], v17 offset:12288\n\t"
"v_mfma_f32_16x16x32_bf16 a[8:11], a[152:155], v[224:227], a[8:11]\n\t"
"buffer_load_dwordx4 v9, s[8:11], s42 offen offset:1024 lds\n\t"
"ds_read_b128 v[128:131], v16 offset:6144\n\t"
"v_mfma_f32_16x16x32_bf16 v[64:67], a[156:159], v[224:227], v[64:67]\n\t"
"ds_read_b128 v[140:143], v16 offset:14336\n\t"
"v_mfma_f32_16x16x32_bf16 v[64:67], a[140:143], v[216:219], v[64:67]\n\t"
"ds_read_b128 v[148:151], v17 offset:6144\n\t"
"v_mfma_f32_16x16x32_bf16 v[68:71], a[140:143], v[228:231], v[68:71]\n\t"
"ds_read_b128 v[164:167], v17 offset:14336\n\t"
"v_mfma_f32_16x16x32_bf16 v[68:71], a[156:159], v[232:235], v[68:71]\n\t"
"buffer_load_dwordx4 v8, s[8:11], s42 offen offset:2048 lds\n\t"
"v_mfma_f32_16x16x32_bf16 a[20:23], a[152:155], v[232:235], a[20:23]\n\t"
"v_mfma_f32_16x16x32_bf16 a[20:23], a[136:139], v[228:231], a[20:23]\n\t"
"v_mov_b32_dpp v172, v100 row_shr:10 row_mask:0xf bank_mask:0x8\n\t"
"v_mfma_f32_16x16x32_bf16 a[16:19], a[132:135], v[228:231], a[16:19]\n\t"
"v_mfma_f32_16x16x32_bf16 a[16:19], a[148:151], v[232:235], a[16:19]\n\t"
"buffer_load_dwordx4 v9, s[8:11], s42 offen offset:3072 lds\n\t"
"v_mov_b32_dpp v173, v101 row_shr:10 row_mask:0xf bank_mask:0x8\n\t"
"v_mfma_f32_16x16x32_bf16 a[12:15], a[144:147], v[232:235], a[12:15]\n\t"
"v_mfma_f32_16x16x32_bf16 a[12:15], a[128:131], v[228:231], a[12:15]\n\t"
"v_mov_b32_dpp v174, v102 row_shr:10 row_mask:0xf bank_mask:0x8\n\t"
"v_mfma_f32_16x16x32_bf16 a[24:27], a[128:131], v[236:239], a[24:27]\n\t"
"s_add_u32 m0, s26, 20480\n\t"
"s_add_u32 s40, s7, 263168\n\t"
"v_mfma_f32_16x16x32_bf16 a[24:27], a[144:147], v[240:243], a[24:27]\n\t"
"buffer_load_dwordx4 v10, s[8:11], s40 offen lds\n\t"
"v_mfma_f32_16x16x32_bf16 a[28:31], a[148:151], v[240:243], a[28:31]\n\t"
"v_mfma_f32_16x16x32_bf16 a[28:31], a[132:135], v[236:239], a[28:31]\n\t"
"v_mov_b32_dpp v175, v103 row_shr:10 row_mask:0xf bank_mask:0x8\n\t"
"v_mfma_f32_16x16x32_bf16 a[32:35], a[136:139], v[236:239], a[32:35]\n\t"
"v_mfma_f32_16x16x32_bf16 a[32:35], a[152:155], v[240:243], a[32:35]\n\t"
"v_mfma_f32_16x16x32_bf16 v[72:75], a[156:159], v[240:243], v[72:75]\n\t"
"v_mfma_f32_16x16x32_bf16 v[72:75], a[140:143], v[236:239], v[72:75]\n\t"
"v_mov_b32_dpp v172, v216 row_shl:1 row_mask:0xf bank_mask:0xf\n\t"
"v_mfma_f32_16x16x32_bf16 v[76:79], a[140:143], v[248:251], v[76:79]\n\t"
"v_mfma_f32_16x16x32_bf16 v[76:79], a[156:159], v[252:255], v[76:79]\n\t"
"v_mfma_f32_16x16x32_bf16 a[44:47], a[152:155], v[252:255], a[44:47]\n\t"
"v_mov_b32_dpp v173, v217 row_shl:1 row_mask:0xf bank_mask:0xf\n\t"
"v_mfma_f32_16x16x32_bf16 a[44:47], a[136:139], v[248:251], a[44:47]\n\t"
"v_mov_b32_dpp v174, v218 row_shl:1 row_mask:0xf bank_mask:0xf\n\t"
"v_mfma_f32_16x16x32_bf16 a[40:43], a[132:135], v[248:251], a[40:43]\n\t"
"v_mov_b32_dpp v175, v219 row_shl:1 row_mask:0xf bank_mask:0xf\n\t"
"v_mfma_f32_16x16x32_bf16 a[40:43], a[148:151], v[252:255], a[40:43]\n\t"
"v_mfma_f32_16x16x32_bf16 a[36:39], a[144:147], v[252:255], a[36:39]\n\t"
"v_mfma_f32_16x16x32_bf16 a[36:39], a[128:131], v[248:251], a[36:39]\n\t"
"v_mfma_f32_16x16x32_bf16 a[48:51], a[128:131], v[40:43], a[48:51]\n\t"
"s_add_u32 m0, s26, 28672\n\t"
"s_add_u32 s41, s6, 557056\n\t"
"v_mfma_f32_16x16x32_bf16 a[48:51], a[144:147], v[48:51], a[48:51]\n\t"
"buffer_load_dwordx4 v4, s[12:15], s41 offen lds\n\t"
"v_mfma_f32_16x16x32_bf16 a[52:55], a[148:151], v[48:51], a[52:55]\n\t"
"ds_read_b64_tr_b16 a[160:161], v12 offset:0\n\t"
"v_mfma_f32_16x16x32_bf16 a[52:55], a[132:135], v[40:43], a[52:55]\n\t"
"ds_read_b64_tr_b16 a[162:163], v12 offset:1024\n\t"
"v_mfma_f32_16x16x32_bf16 a[56:59], a[136:139], v[40:43], a[56:59]\n\t"
"ds_read_b64_tr_b16 a[164:165], v13 offset:0\n\t"
"v_mfma_f32_16x16x32_bf16 a[56:59], a[152:155], v[48:51], a[56:59]\n\t"
"buffer_load_dwordx4 v5, s[12:15], s41 offen offset:1024 lds\n\t"
"v_mfma_f32_16x16x32_bf16 v[80:83], a[156:159], v[48:51], v[80:83]\n\t"
"ds_read_b64_tr_b16 a[166:167], v13 offset:1024\n\t"
"v_mfma_f32_16x16x32_bf16 v[80:83], a[140:143], v[40:43], v[80:83]\n\t"
"ds_read_b64_tr_b16 a[168:169], v14 offset:0\n\t"
"v_mfma_f32_16x16x32_bf16 v[84:87], a[140:143], v[52:55], v[84:87]\n\t"
"ds_read_b64_tr_b16 a[170:171], v14 offset:1024\n\t"
"v_mfma_f32_16x16x32_bf16 v[84:87], a[156:159], v[56:59], v[84:87]\n\t"
"buffer_load_dwordx4 v6, s[12:15], s41 offen offset:2048 lds\n\t"
"v_mfma_f32_16x16x32_bf16 a[68:71], a[152:155], v[56:59], a[68:71]\n\t"
"ds_read_b64_tr_b16 a[172:173], v15 offset:0\n\t"
"v_mfma_f32_16x16x32_bf16 a[68:71], a[136:139], v[52:55], a[68:71]\n\t"
"ds_read_b64_tr_b16 a[174:175], v15 offset:1024\n\t"
"v_mov_b32_dpp v220, v104 row_shr:10 row_mask:0xf bank_mask:0x8\n\t"
"v_mfma_f32_16x16x32_bf16 a[64:67], a[132:135], v[52:55], a[64:67]\n\t"
"s_waitcnt vmcnt(12)\n\t"
"ds_read_b64_tr_b16 a[176:177], v12 offset:8192\n\t"
"v_mfma_f32_16x16x32_bf16 a[64:67], a[148:151], v[56:59], a[64:67]\n\t"
"buffer_load_dwordx4 v7, s[12:15], s41 offen offset:3072 lds\n\t"
"v_mov_b32_dpp v221, v105 row_shr:10 row_mask:0xf bank_mask:0x8\n\t"
"v_mfma_f32_16x16x32_bf16 a[60:63], a[144:147], v[56:59], a[60:63]\n\t"
"ds_read_b64_tr_b16 a[178:179], v12 offset:9216\n\t"
"v_mfma_f32_16x16x32_bf16 a[60:63], a[128:131], v[52:55], a[60:63]\n\t"
"v_mov_b32_dpp v222, v106 row_shr:10 row_mask:0xf bank_mask:0x8\n\t"
"v_mfma_f32_16x16x32_bf16 a[72:75], a[128:131], v[60:63], a[72:75]\n\t"
"s_add_u32 m0, s26, 32768\n\t"
"s_add_u32 s42, s6, 1572864\n\t"
"ds_read_b64_tr_b16 a[180:181], v13 offset:8192\n\t"
"v_mfma_f32_16x16x32_bf16 a[72:75], a[144:147], v[116:119], a[72:75]\n\t"
"buffer_load_dwordx4 v4, s[12:15], s42 offen lds\n\t"
"v_mfma_f32_16x16x32_bf16 a[76:79], a[148:151], v[116:119], a[76:79]\n\t"
"ds_read_b64_tr_b16 a[182:183], v13 offset:9216\n\t"
"v_mfma_f32_16x16x32_bf16 a[76:79], a[132:135], v[60:63], a[76:79]\n\t"
"v_mov_b32_dpp v223, v107 row_shr:10 row_mask:0xf bank_mask:0x8\n\t"
"v_mfma_f32_16x16x32_bf16 a[80:83], a[136:139], v[60:63], a[80:83]\n\t"
"ds_read_b64_tr_b16 a[184:185], v14 offset:8192\n\t"
"v_mfma_f32_16x16x32_bf16 a[80:83], a[152:155], v[116:119], a[80:83]\n\t"
"buffer_load_dwordx4 v5, s[12:15], s42 offen offset:1024 lds\n\t"
"v_mfma_f32_16x16x32_bf16 v[88:91], a[156:159], v[116:119], v[88:91]\n\t"
"ds_read_b64_tr_b16 a[186:187], v14 offset:9216\n\t"
"v_mfma_f32_16x16x32_bf16 v[88:91], a[140:143], v[60:63], v[88:91]\n\t"
"ds_read_b64_tr_b16 a[188:189], v15 offset:8192\n\t"
"v_mov_b32_dpp v220, v224 row_shl:1 row_mask:0xf bank_mask:0xf\n\t"
"v_mfma_f32_16x16x32_bf16 v[92:95], a[140:143], v[120:123], v[92:95]\n\t"
"ds_read_b64_tr_b16 a[190:191], v15 offset:9216\n\t"
"v_mfma_f32_16x16x32_bf16 v[92:95], a[156:159], v[144:147], v[92:95]\n\t"
"buffer_load_dwordx4 v6, s[12:15], s42 offen offset:2048 lds\n\t"
"v_mfma_f32_16x16x32_bf16 a[92:95], a[152:155], v[144:147], a[92:95]\n\t"
"v_mov_b32_dpp v221, v225 row_shl:1 row_mask:0xf bank_mask:0xf\n\t"
"v_mfma_f32_16x16x32_bf16 a[92:95], a[136:139], v[120:123], a[92:95]\n\t"
"v_mov_b32_dpp v222, v226 row_shl:1 row_mask:0xf bank_mask:0xf\n\t"
"v_mfma_f32_16x16x32_bf16 a[88:91], a[132:135], v[120:123], a[88:91]\n\t"
"v_mov_b32_dpp v223, v227 row_shl:1 row_mask:0xf bank_mask:0xf\n\t"
"v_mfma_f32_16x16x32_bf16 a[88:91], a[148:151], v[144:147], a[88:91]\n\t"
"buffer_load_dwordx4 v7, s[12:15], s42 offen offset:3072 lds\n\t"
"v_mfma_f32_16x16x32_bf16 a[84:87], a[144:147], v[144:147], a[84:87]\n\t"
"v_mfma_f32_16x16x32_bf16 a[84:87], a[128:131], v[120:123], a[84:87]\n\t"
"s_waitcnt lgkmcnt(0)\n\t"
"s_waitcnt vmcnt(9)\n\t"
"ds_read_b128 v[244:247], v16 offset:16384\n\t"
"v_mfma_f32_16x16x32_bf16 a[0:3], a[160:163], v[228:231], a[0:3]\n\t"
"s_add_u32 m0, s26, 36864\n\t"
"s_add_u32 s40, s6, 1605632\n\t"
"v_mfma_f32_16x16x32_bf16 a[0:3], a[176:179], v[232:235], a[0:3]\n\t"
"buffer_load_dwordx4 v4, s[12:15], s40 offen lds\n\t"
"v_mfma_f32_16x16x32_bf16 a[4:7], a[180:183], v[232:235], a[4:7]\n\t"
"v_mfma_f32_16x16x32_bf16 a[4:7], a[164:167], v[228:231], a[4:7]\n\t"
"ds_read_b128 v[188:191], v17 offset:16384\n\t"
"v_mfma_f32_16x16x32_bf16 a[8:11], a[168:171], v[228:231], a[8:11]\n\t"
"v_mfma_f32_16x16x32_bf16 a[8:11], a[184:187], v[232:235], a[8:11]\n\t"
"buffer_load_dwordx4 v5, s[12:15], s40 offen offset:1024 lds\n\t"
"ds_read_b128 v[192:195], v16 offset:18432\n\t"
"v_mfma_f32_16x16x32_bf16 v[64:67], a[188:191], v[232:235], v[64:67]\n\t"
"v_mfma_f32_16x16x32_bf16 v[64:67], a[172:175], v[228:231], v[64:67]\n\t"
"ds_read_b128 v[204:207], v17 offset:18432\n\t"
"v_mfma_f32_16x16x32_bf16 v[68:71], a[172:175], v[236:239], v[68:71]\n\t"
"v_mfma_f32_16x16x32_bf16 v[68:71], a[188:191], v[240:243], v[68:71]\n\t"
"buffer_load_dwordx4 v6, s[12:15], s40 offen offset:2048 lds\n\t"
"s_waitcnt vmcnt(11)\n\t"
"ds_read_b128 v[108:111], v16 offset:20480\n\t"
"v_mfma_f32_16x16x32_bf16 a[20:23], a[184:187], v[240:243], a[20:23]\n\t"
"v_mfma_f32_16x16x32_bf16 a[20:23], a[168:171], v[236:239], a[20:23]\n\t"
"ds_read_b128 v[112:115], v17 offset:20480\n\t"
"v_mfma_f32_16x16x32_bf16 a[16:19], a[164:167], v[236:239], a[16:19]\n\t"
"v_mfma_f32_16x16x32_bf16 a[16:19], a[180:183], v[240:243], a[16:19]\n\t"
"buffer_load_dwordx4 v7, s[12:15], s40 offen offset:3072 lds\n\t"
"v_mfma_f32_16x16x32_bf16 a[12:15], a[176:179], v[240:243], a[12:15]\n\t"
"v_mfma_f32_16x16x32_bf16 a[12:15], a[160:163], v[236:239], a[12:15]\n\t"
"v_mfma_f32_16x16x32_bf16 a[24:27], a[160:163], v[248:251], a[24:27]\n\t"
"s_add_u32 m0, s26, 4096\n\t"
"s_add_u32 s41, s6, 2621440\n\t"
"v_mfma_f32_16x16x32_bf16 a[24:27], a[176:179], v[252:255], a[24:27]\n\t"
"buffer_load_dwordx4 v4, s[12:15], s41 offen lds\n\t"
"v_mfma_f32_16x16x32_bf16 a[28:31], a[180:183], v[252:255], a[28:31]\n\t"
"v_mfma_f32_16x16x32_bf16 a[28:31], a[164:167], v[248:251], a[28:31]\n\t"
"v_mfma_f32_16x16x32_bf16 a[32:35], a[168:171], v[248:251], a[32:35]\n\t"
"v_mfma_f32_16x16x32_bf16 a[32:35], a[184:187], v[252:255], a[32:35]\n\t"
"buffer_load_dwordx4 v5, s[12:15], s41 offen offset:1024 lds\n\t"
"v_mfma_f32_16x16x32_bf16 v[72:75], a[188:191], v[252:255], v[72:75]\n\t"
"v_mfma_f32_16x16x32_bf16 v[72:75], a[172:175], v[248:251], v[72:75]\n\t"
"v_mfma_f32_16x16x32_bf16 v[76:79], a[172:175], v[40:43], v[76:79]\n\t"
"v_mfma_f32_16x16x32_bf16 v[76:79], a[188:191], v[48:51], v[76:79]\n\t"
"buffer_load_dwordx4 v6, s[12:15], s41 offen offset:2048 lds\n\t"
"v_mfma_f32_16x16x32_bf16 a[44:47], a[184:187], v[48:51], a[44:47]\n\t"
"v_mfma_f32_16x16x32_bf16 a[44:47], a[168:171], v[40:43], a[44:47]\n\t"
"v_mfma_f32_16x16x32_bf16 a[40:43], a[164:167], v[40:43], a[40:43]\n\t"
"v_mfma_f32_16x16x32_bf16 a[40:43], a[180:183], v[48:51], a[40:43]\n\t"
"buffer_load_dwordx4 v7, s[12:15], s41 offen offset:3072 lds\n\t"
"v_mfma_f32_16x16x32_bf16 a[36:39], a[176:179], v[48:51], a[36:39]\n\t"
"v_mfma_f32_16x16x32_bf16 a[36:39], a[160:163], v[40:43], a[36:39]\n\t"
"s_xor_b32 s43, s33, 2686976\n\t"
"buffer_load_dword v24, v96, s[12:15], s43 offen\n\t"
"v_mfma_f32_16x16x32_bf16 a[48:51], a[160:163], v[52:55], a[48:51]\n\t"
"v_mfma_f32_16x16x32_bf16 a[48:51], a[176:179], v[56:59], a[48:51]\n\t"
"v_mfma_f32_16x16x32_bf16 a[52:55], a[180:183], v[56:59], a[52:55]\n\t"
"ds_read_b64_tr_b16 a[192:193], v12 offset:24576\n\t"
"v_mfma_f32_16x16x32_bf16 a[52:55], a[164:167], v[52:55], a[52:55]\n\t"
"ds_read_b64_tr_b16 a[194:195], v12 offset:25600\n\t"
"v_mfma_f32_16x16x32_bf16 a[56:59], a[168:171], v[52:55], a[56:59]\n\t"
"ds_read_b64_tr_b16 a[196:197], v13 offset:24576\n\t"
"v_mfma_f32_16x16x32_bf16 a[56:59], a[184:187], v[56:59], a[56:59]\n\t"
"v_mfma_f32_16x16x32_bf16 v[80:83], a[188:191], v[56:59], v[80:83]\n\t"
"ds_read_b64_tr_b16 a[198:199], v13 offset:25600\n\t"
"v_mfma_f32_16x16x32_bf16 v[80:83], a[172:175], v[52:55], v[80:83]\n\t"
"ds_read_b64_tr_b16 a[200:201], v14 offset:24576\n\t"
"v_mfma_f32_16x16x32_bf16 v[84:87], a[172:175], v[60:63], v[84:87]\n\t"
"ds_read_b64_tr_b16 a[202:203], v14 offset:25600\n\t"
"v_mfma_f32_16x16x32_bf16 v[84:87], a[188:191], v[116:119], v[84:87]\n\t"
"v_mfma_f32_16x16x32_bf16 a[68:71], a[184:187], v[116:119], a[68:71]\n\t"
"ds_read_b64_tr_b16 a[204:205], v15 offset:24576\n\t"
"v_mfma_f32_16x16x32_bf16 a[68:71], a[168:171], v[60:63], a[68:71]\n\t"
"ds_read_b64_tr_b16 a[206:207], v15 offset:25600\n\t"
"v_mfma_f32_16x16x32_bf16 a[64:67], a[164:167], v[60:63], a[64:67]\n\t"
"s_waitcnt vmcnt(13)\n\t"
"ds_read_b64_tr_b16 a[208:209], v12 offset:28672\n\t"
"v_mfma_f32_16x16x32_bf16 a[64:67], a[180:183], v[116:119], a[64:67]\n\t"
"v_mfma_f32_16x16x32_bf16 a[60:63], a[176:179], v[116:119], a[60:63]\n\t"
"ds_read_b64_tr_b16 a[210:211], v12 offset:29696\n\t"
"v_mfma_f32_16x16x32_bf16 a[60:63], a[160:163], v[60:63], a[60:63]\n\t"
"v_mfma_f32_16x16x32_bf16 a[72:75], a[160:163], v[120:123], a[72:75]\n\t"
"ds_read_b64_tr_b16 a[212:213], v13 offset:28672\n\t"
"v_mfma_f32_16x16x32_bf16 a[72:75], a[176:179], v[144:147], a[72:75]\n\t"
"v_mfma_f32_16x16x32_bf16 a[76:79], a[180:183], v[144:147], a[76:79]\n\t"
"ds_read_b64_tr_b16 a[214:215], v13 offset:29696\n\t"
"v_mfma_f32_16x16x32_bf16 a[76:79], a[164:167], v[120:123], a[76:79]\n\t"
"v_mfma_f32_16x16x32_bf16 a[80:83], a[168:171], v[120:123], a[80:83]\n\t"
"ds_read_b64_tr_b16 a[216:217], v14 offset:28672\n\t"
"v_mfma_f32_16x16x32_bf16 a[80:83], a[184:187], v[144:147], a[80:83]\n\t"
"v_mfma_f32_16x16x32_bf16 v[88:91], a[188:191], v[144:147], v[88:91]\n\t"
"ds_read_b64_tr_b16 a[218:219], v14 offset:29696\n\t"
"v_mfma_f32_16x16x32_bf16 v[88:91], a[172:175], v[120:123], v[88:91]\n\t"
"ds_read_b64_tr_b16 a[220:221], v15 offset:28672\n\t"
"v_mfma_f32_16x16x32_bf16 v[92:95], a[172:175], v[172:175], v[92:95]\n\t"
"ds_read_b64_tr_b16 a[222:223], v15 offset:29696\n\t"
"v_mfma_f32_16x16x32_bf16 v[92:95], a[188:191], v[220:223], v[92:95]\n\t"
"v_mfma_f32_16x16x32_bf16 a[92:95], a[184:187], v[220:223], a[92:95]\n\t"
"v_mfma_f32_16x16x32_bf16 a[92:95], a[168:171], v[172:175], a[92:95]\n\t"
"v_mfma_f32_16x16x32_bf16 a[88:91], a[164:167], v[172:175], a[88:91]\n\t"
"v_mfma_f32_16x16x32_bf16 a[88:91], a[180:183], v[220:223], a[88:91]\n\t"
"v_mfma_f32_16x16x32_bf16 a[84:87], a[176:179], v[220:223], a[84:87]\n\t"
"v_mfma_f32_16x16x32_bf16 a[84:87], a[160:163], v[172:175], a[84:87]\n\t"
"s_xor_b32 s43, s33, 4784128\n\t"
"buffer_load_dword v24, v97, s[12:15], s43 offen\n\t"
"s_xor_b32 s43, s33, 6881280\n\t"
"buffer_load_dword v24, v22, s[12:15], s43 offen\n\t"
"s_waitcnt lgkmcnt(15)\n\t"
"s_waitcnt lgkmcnt(0)\n\t"
"v_mfma_f32_16x16x32_bf16 a[0:3], a[192:195], v[124:127], a[0:3]\n\t"
"v_mfma_f32_16x16x32_bf16 a[0:3], a[208:211], v[152:155], a[0:3]\n\t"
"v_mfma_f32_16x16x32_bf16 a[4:7], a[212:215], v[152:155], a[4:7]\n\t"
"v_mfma_f32_16x16x32_bf16 a[4:7], a[196:199], v[124:127], a[4:7]\n\t"
"v_mfma_f32_16x16x32_bf16 a[8:11], a[200:203], v[124:127], a[8:11]\n\t"
"v_mfma_f32_16x16x32_bf16 a[8:11], a[216:219], v[152:155], a[8:11]\n\t"
"v_mfma_f32_16x16x32_bf16 v[64:67], a[220:223], v[152:155], v[64:67]\n\t"
"v_mfma_f32_16x16x32_bf16 v[64:67], a[204:207], v[124:127], v[64:67]\n\t"
"v_mfma_f32_16x16x32_bf16 v[68:71], a[204:207], v[160:163], v[68:71]\n\t"
"v_mfma_f32_16x16x32_bf16 v[68:71], a[220:223], v[136:139], v[68:71]\n\t"
"v_mfma_f32_16x16x32_bf16 a[20:23], a[216:219], v[136:139], a[20:23]\n\t"
"v_mfma_f32_16x16x32_bf16 a[20:23], a[200:203], v[160:163], a[20:23]\n\t"
"v_mov_b32_dpp v212, v108 row_shr:15 row_mask:0xf bank_mask:0x8\n\t"
"v_mfma_f32_16x16x32_bf16 a[16:19], a[196:199], v[160:163], a[16:19]\n\t"
"v_mfma_f32_16x16x32_bf16 a[16:19], a[212:215], v[136:139], a[16:19]\n\t"
"v_mov_b32_dpp v213, v109 row_shr:15 row_mask:0xf bank_mask:0x8\n\t"
"v_mfma_f32_16x16x32_bf16 a[12:15], a[208:211], v[136:139], a[12:15]\n\t"
"v_mfma_f32_16x16x32_bf16 a[12:15], a[192:195], v[160:163], a[12:15]\n\t"
"v_mov_b32_dpp v214, v110 row_shr:15 row_mask:0xf bank_mask:0x8\n\t"
"v_mfma_f32_16x16x32_bf16 a[24:27], a[192:195], v[168:171], a[24:27]\n\t"
"v_mfma_f32_16x16x32_bf16 a[24:27], a[208:211], v[180:183], a[24:27]\n\t"
"v_mfma_f32_16x16x32_bf16 a[28:31], a[212:215], v[180:183], a[28:31]\n\t"
"v_mfma_f32_16x16x32_bf16 a[28:31], a[196:199], v[168:171], a[28:31]\n\t"
"v_mov_b32_dpp v215, v111 row_shr:15 row_mask:0xf bank_mask:0x8\n\t"
"v_mfma_f32_16x16x32_bf16 a[32:35], a[200:203], v[168:171], a[32:35]\n\t"
"v_mfma_f32_16x16x32_bf16 a[32:35], a[216:219], v[180:183], a[32:35]\n\t"
"v_mfma_f32_16x16x32_bf16 v[72:75], a[220:223], v[180:183], v[72:75]\n\t"
"v_mfma_f32_16x16x32_bf16 v[72:75], a[204:207], v[168:171], v[72:75]\n\t"
"v_mov_b32_dpp v212, v124 row_shl:1 row_mask:0xf bank_mask:0xf\n\t"
"v_mfma_f32_16x16x32_bf16 v[76:79], a[204:207], v[128:131], v[76:79]\n\t"
"v_mfma_f32_16x16x32_bf16 v[76:79], a[220:223], v[148:151], v[76:79]\n\t"
"v_mfma_f32_16x16x32_bf16 a[44:47], a[216:219], v[148:151], a[44:47]\n\t"
"v_mov_b32_dpp v213, v125 row_shl:1 row_mask:0xf bank_mask:0xf\n\t"
"v_mfma_f32_16x16x32_bf16 a[44:47], a[200:203], v[128:131], a[44:47]\n\t"
"v_mov_b32_dpp v214, v126 row_shl:1 row_mask:0xf bank_mask:0xf\n\t"
"v_mfma_f32_16x16x32_bf16 a[40:43], a[196:199], v[128:131], a[40:43]\n\t"
"v_mov_b32_dpp v215, v127 row_shl:1 row_mask:0xf bank_mask:0xf\n\t"
"v_mfma_f32_16x16x32_bf16 a[40:43], a[212:215], v[148:151], a[40:43]\n\t"
"v_mfma_f32_16x16x32_bf16 a[36:39], a[208:211], v[148:151], a[36:39]\n\t"
"v_mfma_f32_16x16x32_bf16 a[36:39], a[192:195], v[128:131], a[36:39]\n\t"
"v_mfma_f32_16x16x32_bf16 a[48:51], a[192:195], v[156:159], a[48:51]\n\t"
"s_add_u32 m0, s26, 12288\n\t"
"s_add_u32 s42, s6, 2654208\n\t"
"v_mfma_f32_16x16x32_bf16 a[48:51], a[208:211], v[132:135], a[48:51]\n\t"
"buffer_load_dwordx4 v4, s[12:15], s42 offen lds\n\t"
"v_mfma_f32_16x16x32_bf16 a[52:55], a[212:215], v[132:135], a[52:55]\n\t"
"s_waitcnt vmcnt(12)\n\t"
"ds_read_b64_tr_b16 a[224:225], v12 offset:32768\n\t"
"v_mfma_f32_16x16x32_bf16 a[52:55], a[196:199], v[156:159], a[52:55]\n\t"
"ds_read_b64_tr_b16 a[226:227], v12 offset:33792\n\t"
"v_mfma_f32_16x16x32_bf16 a[56:59], a[200:203], v[156:159], a[56:59]\n\t"
"ds_read_b64_tr_b16 a[228:229], v13 offset:32768\n\t"
"v_mfma_f32_16x16x32_bf16 a[56:59], a[216:219], v[132:135], a[56:59]\n\t"
"v_mfma_f32_16x16x32_bf16 v[80:83], a[220:223], v[132:135], v[80:83]\n\t"
"ds_read_b64_tr_b16 a[230:231], v13 offset:33792\n\t"
"v_mfma_f32_16x16x32_bf16 v[80:83], a[204:207], v[156:159], v[80:83]\n\t"
"ds_read_b64_tr_b16 a[232:233], v14 offset:32768\n\t"
"v_mfma_f32_16x16x32_bf16 v[84:87], a[204:207], v[140:143], v[84:87]\n\t"
"ds_read_b64_tr_b16 a[234:235], v14 offset:33792\n\t"
"v_mfma_f32_16x16x32_bf16 v[84:87], a[220:223], v[164:167], v[84:87]\n\t"
"buffer_load_dwordx4 v5, s[12:15], s42 offen offset:1024 lds\n\t"
"v_mfma_f32_16x16x32_bf16 a[68:71], a[216:219], v[164:167], a[68:71]\n\t"
"ds_read_b64_tr_b16 a[236:237], v15 offset:32768\n\t"
"v_mfma_f32_16x16x32_bf16 a[68:71], a[200:203], v[140:143], a[68:71]\n\t"
"ds_read_b64_tr_b16 a[238:239], v15 offset:33792\n\t"
"v_mov_b32_dpp v176, v112 row_shr:15 row_mask:0xf bank_mask:0x8\n\t"
"v_mfma_f32_16x16x32_bf16 a[64:67], a[196:199], v[140:143], a[64:67]\n\t"
"s_waitcnt vmcnt(9)\n\t"
"ds_read_b64_tr_b16 a[240:241], v12 offset:36864\n\t"
"v_mfma_f32_16x16x32_bf16 a[64:67], a[212:215], v[164:167], a[64:67]\n\t"
"v_mov_b32_dpp v177, v113 row_shr:15 row_mask:0xf bank_mask:0x8\n\t"
"v_mfma_f32_16x16x32_bf16 a[60:63], a[208:211], v[164:167], a[60:63]\n\t"
"ds_read_b64_tr_b16 a[242:243], v12 offset:37888\n\t"
"v_mfma_f32_16x16x32_bf16 a[60:63], a[192:195], v[140:143], a[60:63]\n\t"
"v_mov_b32_dpp v178, v114 row_shr:15 row_mask:0xf bank_mask:0x8\n\t"
"v_mfma_f32_16x16x32_bf16 a[72:75], a[192:195], v[244:247], a[72:75]\n\t"
"ds_read_b64_tr_b16 a[244:245], v13 offset:36864\n\t"
"v_mfma_f32_16x16x32_bf16 a[72:75], a[208:211], v[188:191], a[72:75]\n\t"
"buffer_load_dwordx4 v6, s[12:15], s42 offen offset:2048 lds\n\t"
"v_mfma_f32_16x16x32_bf16 a[76:79], a[212:215], v[188:191], a[76:79]\n\t"
"ds_read_b64_tr_b16 a[246:247], v13 offset:37888\n\t"
"v_mfma_f32_16x16x32_bf16 a[76:79], a[196:199], v[244:247], a[76:79]\n\t"
"v_mov_b32_dpp v179, v115 row_shr:15 row_mask:0xf bank_mask:0x8\n\t"
"v_mfma_f32_16x16x32_bf16 a[80:83], a[200:203], v[244:247], a[80:83]\n\t"
"ds_read_b64_tr_b16 a[248:249], v14 offset:36864\n\t"
"v_mfma_f32_16x16x32_bf16 a[80:83], a[216:219], v[188:191], a[80:83]\n\t"
"v_mfma_f32_16x16x32_bf16 v[88:91], a[220:223], v[188:191], v[88:91]\n\t"
"ds_read_b64_tr_b16 a[250:251], v14 offset:37888\n\t"
"v_mfma_f32_16x16x32_bf16 v[88:91], a[204:207], v[244:247], v[88:91]\n\t"
"ds_read_b64_tr_b16 a[252:253], v15 offset:36864\n\t"
"v_mov_b32_dpp v176, v152 row_shl:1 row_mask:0xf bank_mask:0xf\n\t"
"v_mfma_f32_16x16x32_bf16 v[92:95], a[204:207], v[192:195], v[92:95]\n\t"
"ds_read_b64_tr_b16 a[254:255], v15 offset:37888\n\t"
"v_mfma_f32_16x16x32_bf16 v[92:95], a[220:223], v[204:207], v[92:95]\n\t"
"buffer_load_dwordx4 v7, s[12:15], s42 offen offset:3072 lds\n\t"
"v_mfma_f32_16x16x32_bf16 a[92:95], a[216:219], v[204:207], a[92:95]\n\t"
"v_mov_b32_dpp v177, v153 row_shl:1 row_mask:0xf bank_mask:0xf\n\t"
"v_mfma_f32_16x16x32_bf16 a[92:95], a[200:203], v[192:195], a[92:95]\n\t"
"v_mov_b32_dpp v178, v154 row_shl:1 row_mask:0xf bank_mask:0xf\n\t"
"v_mfma_f32_16x16x32_bf16 a[88:91], a[196:199], v[192:195], a[88:91]\n\t"
"v_mov_b32_dpp v179, v155 row_shl:1 row_mask:0xf bank_mask:0xf\n\t"
"v_mfma_f32_16x16x32_bf16 a[88:91], a[212:215], v[204:207], a[88:91]\n\t"
"v_mfma_f32_16x16x32_bf16 a[84:87], a[208:211], v[204:207], a[84:87]\n\t"
"v_mfma_f32_16x16x32_bf16 a[84:87], a[192:195], v[192:195], a[84:87]\n\t"
"s_waitcnt lgkmcnt(0)\n\t"
"v_mfma_f32_16x16x32_bf16 a[0:3], a[224:227], v[160:163], a[0:3]\n\t"
"s_add_u32 m0, s26, 0\n\t"
"s_add_u32 s40, s6, 3670016\n\t"
"v_mfma_f32_16x16x32_bf16 a[0:3], a[240:243], v[136:139], a[0:3]\n\t"
"buffer_load_dwordx4 v4, s[12:15], s40 offen lds\n\t"
"v_mfma_f32_16x16x32_bf16 a[4:7], a[244:247], v[136:139], a[4:7]\n\t"
"v_mfma_f32_16x16x32_bf16 a[4:7], a[228:231], v[160:163], a[4:7]\n\t"
"v_mfma_f32_16x16x32_bf16 a[8:11], a[232:235], v[160:163], a[8:11]\n\t"
"v_mfma_f32_16x16x32_bf16 a[8:11], a[248:251], v[136:139], a[8:11]\n\t"
"buffer_load_dwordx4 v5, s[12:15], s40 offen offset:1024 lds\n\t"
"v_mfma_f32_16x16x32_bf16 v[64:67], a[252:255], v[136:139], v[64:67]\n\t"
"v_mfma_f32_16x16x32_bf16 v[64:67], a[236:239], v[160:163], v[64:67]\n\t"
"v_mfma_f32_16x16x32_bf16 v[68:71], a[236:239], v[168:171], v[68:71]\n\t"
"v_mfma_f32_16x16x32_bf16 v[68:71], a[252:255], v[180:183], v[68:71]\n\t"
"buffer_load_dwordx4 v6, s[12:15], s40 offen offset:2048 lds\n\t"
"v_mfma_f32_16x16x32_bf16 a[20:23], a[248:251], v[180:183], a[20:23]\n\t"
"v_mfma_f32_16x16x32_bf16 a[20:23], a[232:235], v[168:171], a[20:23]\n\t"
"v_mov_b32_dpp v184, v108 row_shr:14 row_mask:0xf bank_mask:0x8\n\t"
"v_mfma_f32_16x16x32_bf16 a[16:19], a[228:231], v[168:171], a[16:19]\n\t"
"v_mfma_f32_16x16x32_bf16 a[16:19], a[244:247], v[180:183], a[16:19]\n\t"
"buffer_load_dwordx4 v7, s[12:15], s40 offen offset:3072 lds\n\t"
"v_mov_b32_dpp v185, v109 row_shr:14 row_mask:0xf bank_mask:0x8\n\t"
"v_mfma_f32_16x16x32_bf16 a[12:15], a[240:243], v[180:183], a[12:15]\n\t"
"v_mfma_f32_16x16x32_bf16 a[12:15], a[224:227], v[168:171], a[12:15]\n\t"
"v_mov_b32_dpp v186, v110 row_shr:14 row_mask:0xf bank_mask:0x8\n\t"
"v_mfma_f32_16x16x32_bf16 a[24:27], a[224:227], v[128:131], a[24:27]\n\t"
"s_add_u32 m0, s26, 8192\n\t"
"s_add_u32 s41, s6, 3702784\n\t"
"v_mfma_f32_16x16x32_bf16 a[24:27], a[240:243], v[148:151], a[24:27]\n\t"
"buffer_load_dwordx4 v4, s[12:15], s41 offen lds\n\t"
"v_mfma_f32_16x16x32_bf16 a[28:31], a[244:247], v[148:151], a[28:31]\n\t"
"v_mfma_f32_16x16x32_bf16 a[28:31], a[228:231], v[128:131], a[28:31]\n\t"
"v_mov_b32_dpp v187, v111 row_shr:14 row_mask:0xf bank_mask:0x8\n\t"
"v_mfma_f32_16x16x32_bf16 a[32:35], a[232:235], v[128:131], a[32:35]\n\t"
"v_mfma_f32_16x16x32_bf16 a[32:35], a[248:251], v[148:151], a[32:35]\n\t"
"buffer_load_dwordx4 v5, s[12:15], s41 offen offset:1024 lds\n\t"
"v_mfma_f32_16x16x32_bf16 v[72:75], a[252:255], v[148:151], v[72:75]\n\t"
"v_mfma_f32_16x16x32_bf16 v[72:75], a[236:239], v[128:131], v[72:75]\n\t"
"v_mov_b32_dpp v184, v160 row_shl:1 row_mask:0xf bank_mask:0xf\n\t"
"v_mfma_f32_16x16x32_bf16 v[76:79], a[236:239], v[156:159], v[76:79]\n\t"
"v_mfma_f32_16x16x32_bf16 v[76:79], a[252:255], v[132:135], v[76:79]\n\t"
"buffer_load_dwordx4 v6, s[12:15], s41 offen offset:2048 lds\n\t"
"v_mfma_f32_16x16x32_bf16 a[44:47], a[248:251], v[132:135], a[44:47]\n\t"
"v_mov_b32_dpp v185, v161 row_shl:1 row_mask:0xf bank_mask:0xf\n\t"
"v_mfma_f32_16x16x32_bf16 a[44:47], a[232:235], v[156:159], a[44:47]\n\t"
"v_mov_b32_dpp v186, v162 row_shl:1 row_mask:0xf bank_mask:0xf\n\t"
"v_mfma_f32_16x16x32_bf16 a[40:43], a[228:231], v[156:159], a[40:43]\n\t"
"v_mov_b32_dpp v187, v163 row_shl:1 row_mask:0xf bank_mask:0xf\n\t"
"v_mfma_f32_16x16x32_bf16 a[40:43], a[244:247], v[132:135], a[40:43]\n\t"
"buffer_load_dwordx4 v7, s[12:15], s41 offen offset:3072 lds\n\t"
"v_mfma_f32_16x16x32_bf16 a[36:39], a[240:243], v[132:135], a[36:39]\n\t"
"v_mfma_f32_16x16x32_bf16 a[36:39], a[224:227], v[156:159], a[36:39]\n\t"
"v_mfma_f32_16x16x32_bf16 a[48:51], a[224:227], v[140:143], a[48:51]\n\t"
"s_add_u32 m0, s26, 16384\n\t"
"s_add_u32 s42, s6, 4718592\n\t"
"v_mfma_f32_16x16x32_bf16 a[48:51], a[240:243], v[164:167], a[48:51]\n\t"
"buffer_load_dwordx4 v4, s[12:15], s42 offen lds\n\t"
"v_mfma_f32_16x16x32_bf16 a[52:55], a[244:247], v[164:167], a[52:55]\n\t"
"s_waitcnt vmcnt(16)\n\t"
"ds_read_b64_tr_b16 a[96:97], v12 offset:4096\n\t"
"v_mfma_f32_16x16x32_bf16 a[52:55], a[228:231], v[140:143], a[52:55]\n\t"
"ds_read_b64_tr_b16 a[98:99], v12 offset:5120\n\t"
"v_mfma_f32_16x16x32_bf16 a[56:59], a[232:235], v[140:143], a[56:59]\n\t"
"ds_read_b64_tr_b16 a[100:101], v13 offset:4096\n\t"
"v_mfma_f32_16x16x32_bf16 a[56:59], a[248:251], v[164:167], a[56:59]\n\t"
"buffer_load_dwordx4 v5, s[12:15], s42 offen offset:1024 lds\n\t"
"v_mfma_f32_16x16x32_bf16 v[80:83], a[252:255], v[164:167], v[80:83]\n\t"
"ds_read_b64_tr_b16 a[102:103], v13 offset:5120\n\t"
"v_mfma_f32_16x16x32_bf16 v[80:83], a[236:239], v[140:143], v[80:83]\n\t"
"ds_read_b64_tr_b16 a[104:105], v14 offset:4096\n\t"
"v_mfma_f32_16x16x32_bf16 v[84:87], a[236:239], v[244:247], v[84:87]\n\t"
"ds_read_b64_tr_b16 a[106:107], v14 offset:5120\n\t"
"v_mfma_f32_16x16x32_bf16 v[84:87], a[252:255], v[188:191], v[84:87]\n\t"
"buffer_load_dwordx4 v6, s[12:15], s42 offen offset:2048 lds\n\t"
"v_mfma_f32_16x16x32_bf16 a[68:71], a[248:251], v[188:191], a[68:71]\n\t"
"ds_read_b64_tr_b16 a[108:109], v15 offset:4096\n\t"
"v_mfma_f32_16x16x32_bf16 a[68:71], a[232:235], v[244:247], a[68:71]\n\t"
"ds_read_b64_tr_b16 a[110:111], v15 offset:5120\n\t"
"v_mov_b32_dpp v196, v112 row_shr:14 row_mask:0xf bank_mask:0x8\n\t"
"v_mfma_f32_16x16x32_bf16 a[64:67], a[228:231], v[244:247], a[64:67]\n\t"
"s_waitcnt vmcnt(11)\n\t"
"ds_read_b64_tr_b16 a[112:113], v12 offset:12288\n\t"
"v_mfma_f32_16x16x32_bf16 a[64:67], a[244:247], v[188:191], a[64:67]\n\t"
"buffer_load_dwordx4 v7, s[12:15], s42 offen offset:3072 lds\n\t"
"v_mov_b32_dpp v197, v113 row_shr:14 row_mask:0xf bank_mask:0x8\n\t"
"v_mfma_f32_16x16x32_bf16 a[60:63], a[240:243], v[188:191], a[60:63]\n\t"
"ds_read_b64_tr_b16 a[114:115], v12 offset:13312\n\t"
"v_mfma_f32_16x16x32_bf16 a[60:63], a[224:227], v[244:247], a[60:63]\n\t"
"v_mov_b32_dpp v198, v114 row_shr:14 row_mask:0xf bank_mask:0x8\n\t"
"v_mfma_f32_16x16x32_bf16 a[72:75], a[224:227], v[192:195], a[72:75]\n\t"
"s_add_u32 m0, s26, 20480\n\t"
"s_add_u32 s40, s6, 4751360\n\t"
"ds_read_b64_tr_b16 a[116:117], v13 offset:12288\n\t"
"v_mfma_f32_16x16x32_bf16 a[72:75], a[240:243], v[204:207], a[72:75]\n\t"
"buffer_load_dwordx4 v4, s[12:15], s40 offen lds\n\t"
"v_mfma_f32_16x16x32_bf16 a[76:79], a[244:247], v[204:207], a[76:79]\n\t"
"ds_read_b64_tr_b16 a[118:119], v13 offset:13312\n\t"
"v_mfma_f32_16x16x32_bf16 a[76:79], a[228:231], v[192:195], a[76:79]\n\t"
"v_mov_b32_dpp v199, v115 row_shr:14 row_mask:0xf bank_mask:0x8\n\t"
"v_mfma_f32_16x16x32_bf16 a[80:83], a[232:235], v[192:195], a[80:83]\n\t"
"ds_read_b64_tr_b16 a[120:121], v14 offset:12288\n\t"
"v_mfma_f32_16x16x32_bf16 a[80:83], a[248:251], v[204:207], a[80:83]\n\t"
"buffer_load_dwordx4 v5, s[12:15], s40 offen offset:1024 lds\n\t"
"v_mfma_f32_16x16x32_bf16 v[88:91], a[252:255], v[204:207], v[88:91]\n\t"
"ds_read_b64_tr_b16 a[122:123], v14 offset:13312\n\t"
"v_mfma_f32_16x16x32_bf16 v[88:91], a[236:239], v[192:195], v[88:91]\n\t"
"ds_read_b64_tr_b16 a[124:125], v15 offset:12288\n\t"
"v_mov_b32_dpp v196, v136 row_shl:1 row_mask:0xf bank_mask:0xf\n\t"
"v_mfma_f32_16x16x32_bf16 v[92:95], a[236:239], v[212:215], v[92:95]\n\t"
"ds_read_b64_tr_b16 a[126:127], v15 offset:13312\n\t"
"v_mfma_f32_16x16x32_bf16 v[92:95], a[252:255], v[176:179], v[92:95]\n\t"
"buffer_load_dwordx4 v6, s[12:15], s40 offen offset:2048 lds\n\t"
"v_mfma_f32_16x16x32_bf16 a[92:95], a[248:251], v[176:179], a[92:95]\n\t"
"v_mov_b32_dpp v197, v137 row_shl:1 row_mask:0xf bank_mask:0xf\n\t"
"v_mfma_f32_16x16x32_bf16 a[92:95], a[232:235], v[212:215], a[92:95]\n\t"
"v_mov_b32_dpp v198, v138 row_shl:1 row_mask:0xf bank_mask:0xf\n\t"
"v_mfma_f32_16x16x32_bf16 a[88:91], a[228:231], v[212:215], a[88:91]\n\t"
"v_mov_b32_dpp v199, v139 row_shl:1 row_mask:0xf bank_mask:0xf\n\t"
"v_mfma_f32_16x16x32_bf16 a[88:91], a[244:247], v[176:179], a[88:91]\n\t"
"buffer_load_dwordx4 v7, s[12:15], s40 offen offset:3072 lds\n\t"
"v_mfma_f32_16x16x32_bf16 a[84:87], a[240:243], v[176:179], a[84:87]\n\t"
"v_mfma_f32_16x16x32_bf16 a[84:87], a[224:227], v[212:215], a[84:87]\n\t"
"s_waitcnt lgkmcnt(0)\n\t"
"v_mfma_f32_16x16x32_bf16 a[0:3], a[96:99], v[168:171], a[0:3]\n\t"
"s_add_u32 m0, s26, 24576\n\t"
"s_add_u32 s41, s6, 5767168\n\t"
"v_mfma_f32_16x16x32_bf16 a[0:3], a[112:115], v[180:183], a[0:3]\n\t"
"buffer_load_dwordx4 v4, s[12:15], s41 offen lds\n\t"
"v_mfma_f32_16x16x32_bf16 a[4:7], a[116:119], v[180:183], a[4:7]\n\t"
"v_mfma_f32_16x16x32_bf16 a[4:7], a[100:103], v[168:171], a[4:7]\n\t"
"v_mfma_f32_16x16x32_bf16 a[8:11], a[104:107], v[168:171], a[8:11]\n\t"
"v_mfma_f32_16x16x32_bf16 a[8:11], a[120:123], v[180:183], a[8:11]\n\t"
"buffer_load_dwordx4 v5, s[12:15], s41 offen offset:1024 lds\n\t"
"v_mfma_f32_16x16x32_bf16 v[64:67], a[124:127], v[180:183], v[64:67]\n\t"
"v_mfma_f32_16x16x32_bf16 v[64:67], a[108:111], v[168:171], v[64:67]\n\t"
"v_mfma_f32_16x16x32_bf16 v[68:71], a[108:111], v[128:131], v[68:71]\n\t"
"v_mfma_f32_16x16x32_bf16 v[68:71], a[124:127], v[148:151], v[68:71]\n\t"
"buffer_load_dwordx4 v6, s[12:15], s41 offen offset:2048 lds\n\t"
"v_mfma_f32_16x16x32_bf16 a[20:23], a[120:123], v[148:151], a[20:23]\n\t"
"v_mfma_f32_16x16x32_bf16 a[20:23], a[104:107], v[128:131], a[20:23]\n\t"
"v_mov_b32_dpp v200, v108 row_shr:13 row_mask:0xf bank_mask:0x8\n\t"
"v_mfma_f32_16x16x32_bf16 a[16:19], a[100:103], v[128:131], a[16:19]\n\t"
"v_mfma_f32_16x16x32_bf16 a[16:19], a[116:119], v[148:151], a[16:19]\n\t"
"buffer_load_dwordx4 v7, s[12:15], s41 offen offset:3072 lds\n\t"
"v_mov_b32_dpp v201, v109 row_shr:13 row_mask:0xf bank_mask:0x8\n\t"
"v_mfma_f32_16x16x32_bf16 a[12:15], a[112:115], v[148:151], a[12:15]\n\t"
"v_mfma_f32_16x16x32_bf16 a[12:15], a[96:99], v[128:131], a[12:15]\n\t"
"v_mov_b32_dpp v202, v110 row_shr:13 row_mask:0xf bank_mask:0x8\n\t"
"v_mfma_f32_16x16x32_bf16 a[24:27], a[96:99], v[156:159], a[24:27]\n\t"
"s_add_u32 m0, s26, 28672\n\t"
"s_add_u32 s42, s6, 5799936\n\t"
"v_mfma_f32_16x16x32_bf16 a[24:27], a[112:115], v[132:135], a[24:27]\n\t"
"buffer_load_dwordx4 v4, s[12:15], s42 offen lds\n\t"
"v_mfma_f32_16x16x32_bf16 a[28:31], a[116:119], v[132:135], a[28:31]\n\t"
"v_mfma_f32_16x16x32_bf16 a[28:31], a[100:103], v[156:159], a[28:31]\n\t"
"v_mov_b32_dpp v203, v111 row_shr:13 row_mask:0xf bank_mask:0x8\n\t"
"v_mfma_f32_16x16x32_bf16 a[32:35], a[104:107], v[156:159], a[32:35]\n\t"
"v_mfma_f32_16x16x32_bf16 a[32:35], a[120:123], v[132:135], a[32:35]\n\t"
"buffer_load_dwordx4 v5, s[12:15], s42 offen offset:1024 lds\n\t"
"v_mfma_f32_16x16x32_bf16 v[72:75], a[124:127], v[132:135], v[72:75]\n\t"
"v_mfma_f32_16x16x32_bf16 v[72:75], a[108:111], v[156:159], v[72:75]\n\t"
"v_mov_b32_dpp v200, v168 row_shl:1 row_mask:0xf bank_mask:0xf\n\t"
"v_mfma_f32_16x16x32_bf16 v[76:79], a[108:111], v[140:143], v[76:79]\n\t"
"v_mfma_f32_16x16x32_bf16 v[76:79], a[124:127], v[164:167], v[76:79]\n\t"
"buffer_load_dwordx4 v6, s[12:15], s42 offen offset:2048 lds\n\t"
"v_mfma_f32_16x16x32_bf16 a[44:47], a[120:123], v[164:167], a[44:47]\n\t"
"v_mov_b32_dpp v201, v169 row_shl:1 row_mask:0xf bank_mask:0xf\n\t"
"v_mfma_f32_16x16x32_bf16 a[44:47], a[104:107], v[140:143], a[44:47]\n\t"
"v_mov_b32_dpp v202, v170 row_shl:1 row_mask:0xf bank_mask:0xf\n\t"
"v_mfma_f32_16x16x32_bf16 a[40:43], a[100:103], v[140:143], a[40:43]\n\t"
"v_mov_b32_dpp v203, v171 row_shl:1 row_mask:0xf bank_mask:0xf\n\t"
"v_mfma_f32_16x16x32_bf16 a[40:43], a[116:119], v[164:167], a[40:43]\n\t"
"buffer_load_dwordx4 v7, s[12:15], s42 offen offset:3072 lds\n\t"
"v_mfma_f32_16x16x32_bf16 a[36:39], a[112:115], v[164:167], a[36:39]\n\t"
"v_mfma_f32_16x16x32_bf16 a[36:39], a[96:99], v[140:143], a[36:39]\n\t"
"v_mfma_f32_16x16x32_bf16 a[48:51], a[96:99], v[244:247], a[48:51]\n\t"
"s_add_u32 m0, s26, 32768\n\t"
"s_add_u32 s40, s6, 6815744\n\t"
"v_mfma_f32_16x16x32_bf16 a[48:51], a[112:115], v[188:191], a[48:51]\n\t"
"buffer_load_dwordx4 v4, s[12:15], s40 offen lds\n\t"
"v_mfma_f32_16x16x32_bf16 a[52:55], a[116:119], v[188:191], a[52:55]\n\t"
"s_waitcnt vmcnt(21)\n\t"
"ds_read_b64_tr_b16 a[128:129], v12 offset:0\n\t"
"v_mfma_f32_16x16x32_bf16 a[52:55], a[100:103], v[244:247], a[52:55]\n\t"
"ds_read_b64_tr_b16 a[130:131], v12 offset:1024\n\t"
"v_mfma_f32_16x16x32_bf16 a[56:59], a[104:107], v[244:247], a[56:59]\n\t"
"ds_read_b64_tr_b16 a[132:133], v13 offset:0\n\t"
"v_mfma_f32_16x16x32_bf16 a[56:59], a[120:123], v[188:191], a[56:59]\n\t"
"buffer_load_dwordx4 v5, s[12:15], s40 offen offset:1024 lds\n\t"
"v_mfma_f32_16x16x32_bf16 v[80:83], a[124:127], v[188:191], v[80:83]\n\t"
"ds_read_b64_tr_b16 a[134:135], v13 offset:1024\n\t"
"v_mfma_f32_16x16x32_bf16 v[80:83], a[108:111], v[244:247], v[80:83]\n\t"
"ds_read_b64_tr_b16 a[136:137], v14 offset:0\n\t"
"v_mfma_f32_16x16x32_bf16 v[84:87], a[108:111], v[192:195], v[84:87]\n\t"
"ds_read_b64_tr_b16 a[138:139], v14 offset:1024\n\t"
"v_mfma_f32_16x16x32_bf16 v[84:87], a[124:127], v[204:207], v[84:87]\n\t"
"buffer_load_dwordx4 v6, s[12:15], s40 offen offset:2048 lds\n\t"
"v_mfma_f32_16x16x32_bf16 a[68:71], a[120:123], v[204:207], a[68:71]\n\t"
"ds_read_b64_tr_b16 a[140:141], v15 offset:0\n\t"
"v_mfma_f32_16x16x32_bf16 a[68:71], a[104:107], v[192:195], a[68:71]\n\t"
"ds_read_b64_tr_b16 a[142:143], v15 offset:1024\n\t"
"v_mov_b32_dpp v208, v112 row_shr:13 row_mask:0xf bank_mask:0x8\n\t"
"v_mfma_f32_16x16x32_bf16 a[64:67], a[100:103], v[192:195], a[64:67]\n\t"
"s_waitcnt vmcnt(19)\n\t"
"ds_read_b64_tr_b16 a[144:145], v12 offset:8192\n\t"
"v_mfma_f32_16x16x32_bf16 a[64:67], a[116:119], v[204:207], a[64:67]\n\t"
"buffer_load_dwordx4 v7, s[12:15], s40 offen offset:3072 lds\n\t"
"v_mov_b32_dpp v209, v113 row_shr:13 row_mask:0xf bank_mask:0x8\n\t"
"v_mfma_f32_16x16x32_bf16 a[60:63], a[112:115], v[204:207], a[60:63]\n\t"
"ds_read_b64_tr_b16 a[146:147], v12 offset:9216\n\t"
"v_mfma_f32_16x16x32_bf16 a[60:63], a[96:99], v[192:195], a[60:63]\n\t"
"v_mov_b32_dpp v210, v114 row_shr:13 row_mask:0xf bank_mask:0x8\n\t"
"v_mfma_f32_16x16x32_bf16 a[72:75], a[96:99], v[212:215], a[72:75]\n\t"
"s_add_u32 m0, s26, 36864\n\t"
"s_add_u32 s41, s6, 6848512\n\t"
"ds_read_b64_tr_b16 a[148:149], v13 offset:8192\n\t"
"v_mfma_f32_16x16x32_bf16 a[72:75], a[112:115], v[176:179], a[72:75]\n\t"
"buffer_load_dwordx4 v4, s[12:15], s41 offen lds\n\t"
"v_mfma_f32_16x16x32_bf16 a[76:79], a[116:119], v[176:179], a[76:79]\n\t"
"ds_read_b64_tr_b16 a[150:151], v13 offset:9216\n\t"
"v_mfma_f32_16x16x32_bf16 a[76:79], a[100:103], v[212:215], a[76:79]\n\t"
"v_mov_b32_dpp v211, v115 row_shr:13 row_mask:0xf bank_mask:0x8\n\t"
"v_mfma_f32_16x16x32_bf16 a[80:83], a[104:107], v[212:215], a[80:83]\n\t"
"ds_read_b64_tr_b16 a[152:153], v14 offset:8192\n\t"
"v_mfma_f32_16x16x32_bf16 a[80:83], a[120:123], v[176:179], a[80:83]\n\t"
"buffer_load_dwordx4 v5, s[12:15], s41 offen offset:1024 lds\n\t"
"v_mfma_f32_16x16x32_bf16 v[88:91], a[124:127], v[176:179], v[88:91]\n\t"
"ds_read_b64_tr_b16 a[154:155], v14 offset:9216\n\t"
"v_mfma_f32_16x16x32_bf16 v[88:91], a[108:111], v[212:215], v[88:91]\n\t"
"ds_read_b64_tr_b16 a[156:157], v15 offset:8192\n\t"
"v_mov_b32_dpp v208, v180 row_shl:1 row_mask:0xf bank_mask:0xf\n\t"
"v_mfma_f32_16x16x32_bf16 v[92:95], a[108:111], v[184:187], v[92:95]\n\t"
"ds_read_b64_tr_b16 a[158:159], v15 offset:9216\n\t"
"v_mfma_f32_16x16x32_bf16 v[92:95], a[124:127], v[196:199], v[92:95]\n\t"
"buffer_load_dwordx4 v6, s[12:15], s41 offen offset:2048 lds\n\t"
"v_mfma_f32_16x16x32_bf16 a[92:95], a[120:123], v[196:199], a[92:95]\n\t"
"v_mov_b32_dpp v209, v181 row_shl:1 row_mask:0xf bank_mask:0xf\n\t"
"v_mfma_f32_16x16x32_bf16 a[92:95], a[104:107], v[184:187], a[92:95]\n\t"
"v_mov_b32_dpp v210, v182 row_shl:1 row_mask:0xf bank_mask:0xf\n\t"
"v_mfma_f32_16x16x32_bf16 a[88:91], a[100:103], v[184:187], a[88:91]\n\t"
"v_mov_b32_dpp v211, v183 row_shl:1 row_mask:0xf bank_mask:0xf\n\t"
"v_mfma_f32_16x16x32_bf16 a[88:91], a[116:119], v[196:199], a[88:91]\n\t"
"buffer_load_dwordx4 v7, s[12:15], s41 offen offset:3072 lds\n\t"
"v_mfma_f32_16x16x32_bf16 a[84:87], a[112:115], v[196:199], a[84:87]\n\t"
"v_mfma_f32_16x16x32_bf16 a[84:87], a[96:99], v[184:187], a[84:87]\n\t"
"s_waitcnt lgkmcnt(0)\n\t"
"v_mfma_f32_16x16x32_bf16 a[0:3], a[128:131], v[128:131], a[0:3]\n\t"
"v_mfma_f32_16x16x32_bf16 a[0:3], a[144:147], v[148:151], a[0:3]\n\t"
"v_mfma_f32_16x16x32_bf16 a[4:7], a[148:151], v[148:151], a[4:7]\n\t"
"v_mfma_f32_16x16x32_bf16 a[4:7], a[132:135], v[128:131], a[4:7]\n\t"
"v_mfma_f32_16x16x32_bf16 a[8:11], a[136:139], v[128:131], a[8:11]\n\t"
"v_mfma_f32_16x16x32_bf16 a[8:11], a[152:155], v[148:151], a[8:11]\n\t"
"v_mfma_f32_16x16x32_bf16 v[64:67], a[156:159], v[148:151], v[64:67]\n\t"
"v_mfma_f32_16x16x32_bf16 v[64:67], a[140:143], v[128:131], v[64:67]\n\t"
"v_mfma_f32_16x16x32_bf16 v[68:71], a[140:143], v[156:159], v[68:71]\n\t"
"s_waitcnt vmcnt(20)\n\t"
"ds_read_b64_tr_b16 a[160:161], v12 offset:16384\n\t"
"v_mfma_f32_16x16x32_bf16 v[68:71], a[156:159], v[132:135], v[68:71]\n\t"
"v_mfma_f32_16x16x32_bf16 a[20:23], a[152:155], v[132:135], a[20:23]\n\t"
"ds_read_b64_tr_b16 a[162:163], v12 offset:17408\n\t"
"v_mfma_f32_16x16x32_bf16 a[20:23], a[136:139], v[156:159], a[20:23]\n\t"
"v_mov_b32_dpp v216, v108 row_shr:12 row_mask:0xf bank_mask:0x8\n\t"
"v_mfma_f32_16x16x32_bf16 a[16:19], a[132:135], v[156:159], a[16:19]\n\t"
"s_waitcnt vmcnt(16)\n\t"
"ds_read_b64_tr_b16 a[176:177], v12 offset:20480\n\t"
"v_mfma_f32_16x16x32_bf16 a[16:19], a[148:151], v[132:135], a[16:19]\n\t"
"v_mov_b32_dpp v217, v109 row_shr:12 row_mask:0xf bank_mask:0x8\n\t"
"v_mfma_f32_16x16x32_bf16 a[12:15], a[144:147], v[132:135], a[12:15]\n\t"
"ds_read_b64_tr_b16 a[178:179], v12 offset:21504\n\t"
"v_mfma_f32_16x16x32_bf16 a[12:15], a[128:131], v[156:159], a[12:15]\n\t"
"v_mov_b32_dpp v218, v110 row_shr:12 row_mask:0xf bank_mask:0x8\n\t"
"v_mfma_f32_16x16x32_bf16 a[24:27], a[128:131], v[140:143], a[24:27]\n\t"
"s_waitcnt vmcnt(12)\n\t"
"ds_read_b64_tr_b16 a[192:193], v12 offset:24576\n\t"
"v_mfma_f32_16x16x32_bf16 a[24:27], a[144:147], v[164:167], a[24:27]\n\t"
"v_mfma_f32_16x16x32_bf16 a[28:31], a[148:151], v[164:167], a[28:31]\n\t"
"ds_read_b64_tr_b16 a[194:195], v12 offset:25600\n\t"
"v_mfma_f32_16x16x32_bf16 a[28:31], a[132:135], v[140:143], a[28:31]\n\t"
"v_mov_b32_dpp v219, v111 row_shr:12 row_mask:0xf bank_mask:0x8\n\t"
"v_mfma_f32_16x16x32_bf16 a[32:35], a[136:139], v[140:143], a[32:35]\n\t"
"s_waitcnt vmcnt(8)\n\t"
"ds_read_b64_tr_b16 a[208:209], v12 offset:28672\n\t"
"v_mfma_f32_16x16x32_bf16 a[32:35], a[152:155], v[164:167], a[32:35]\n\t"
"v_mfma_f32_16x16x32_bf16 v[72:75], a[156:159], v[164:167], v[72:75]\n\t"
"ds_read_b64_tr_b16 a[210:211], v12 offset:29696\n\t"
"v_mfma_f32_16x16x32_bf16 v[72:75], a[140:143], v[140:143], v[72:75]\n\t"
"v_mov_b32_dpp v216, v128 row_shl:1 row_mask:0xf bank_mask:0xf\n\t"
"v_mfma_f32_16x16x32_bf16 v[76:79], a[140:143], v[244:247], v[76:79]\n\t"
"s_waitcnt vmcnt(4)\n\t"
"ds_read_b64_tr_b16 a[224:225], v12 offset:32768\n\t"
"v_mfma_f32_16x16x32_bf16 v[76:79], a[156:159], v[188:191], v[76:79]\n\t"
"v_mfma_f32_16x16x32_bf16 a[44:47], a[152:155], v[188:191], a[44:47]\n\t"
"ds_read_b64_tr_b16 a[226:227], v12 offset:33792\n\t"
"v_mov_b32_dpp v217, v129 row_shl:1 row_mask:0xf bank_mask:0xf\n\t"
"v_mfma_f32_16x16x32_bf16 a[44:47], a[136:139], v[244:247], a[44:47]\n\t"
"s_waitcnt vmcnt(0)\n\t"
"ds_read_b64_tr_b16 a[240:241], v12 offset:36864\n\t"
"v_mov_b32_dpp v218, v130 row_shl:1 row_mask:0xf bank_mask:0xf\n\t"
"v_mfma_f32_16x16x32_bf16 a[40:43], a[132:135], v[244:247], a[40:43]\n\t"
"ds_read_b64_tr_b16 a[242:243], v12 offset:37888\n\t"
"v_mov_b32_dpp v219, v131 row_shl:1 row_mask:0xf bank_mask:0xf\n\t"
"v_mfma_f32_16x16x32_bf16 a[40:43], a[148:151], v[188:191], a[40:43]\n\t"
"v_mfma_f32_16x16x32_bf16 a[36:39], a[144:147], v[188:191], a[36:39]\n\t"
"v_mfma_f32_16x16x32_bf16 a[36:39], a[128:131], v[244:247], a[36:39]\n\t"
"v_mfma_f32_16x16x32_bf16 a[48:51], a[128:131], v[192:195], a[48:51]\n\t"
"v_mfma_f32_16x16x32_bf16 a[48:51], a[144:147], v[204:207], a[48:51]\n\t"
"v_mfma_f32_16x16x32_bf16 a[52:55], a[148:151], v[204:207], a[52:55]\n\t"
"v_mfma_f32_16x16x32_bf16 a[52:55], a[132:135], v[192:195], a[52:55]\n\t"
"v_mfma_f32_16x16x32_bf16 a[56:59], a[136:139], v[192:195], a[56:59]\n\t"
"v_mfma_f32_16x16x32_bf16 a[56:59], a[152:155], v[204:207], a[56:59]\n\t"
"v_mfma_f32_16x16x32_bf16 v[80:83], a[156:159], v[204:207], v[80:83]\n\t"
"v_mfma_f32_16x16x32_bf16 v[80:83], a[140:143], v[192:195], v[80:83]\n\t"
"v_mfma_f32_16x16x32_bf16 v[84:87], a[140:143], v[212:215], v[84:87]\n\t"
"ds_read_b64_tr_b16 a[164:165], v13 offset:16384\n\t"
"v_mfma_f32_16x16x32_bf16 v[84:87], a[156:159], v[176:179], v[84:87]\n\t"
"v_mfma_f32_16x16x32_bf16 a[68:71], a[152:155], v[176:179], a[68:71]\n\t"
"ds_read_b64_tr_b16 a[166:167], v13 offset:17408\n\t"
"v_mfma_f32_16x16x32_bf16 a[68:71], a[136:139], v[212:215], a[68:71]\n\t"
"v_mov_b32_dpp v224, v112 row_shr:12 row_mask:0xf bank_mask:0x8\n\t"
"v_mfma_f32_16x16x32_bf16 a[64:67], a[132:135], v[212:215], a[64:67]\n\t"
"ds_read_b64_tr_b16 a[180:181], v13 offset:20480\n\t"
"v_mfma_f32_16x16x32_bf16 a[64:67], a[148:151], v[176:179], a[64:67]\n\t"
"v_mov_b32_dpp v225, v113 row_shr:12 row_mask:0xf bank_mask:0x8\n\t"
"v_mfma_f32_16x16x32_bf16 a[60:63], a[144:147], v[176:179], a[60:63]\n\t"
"ds_read_b64_tr_b16 a[182:183], v13 offset:21504\n\t"
"v_mfma_f32_16x16x32_bf16 a[60:63], a[128:131], v[212:215], a[60:63]\n\t"
"v_mov_b32_dpp v226, v114 row_shr:12 row_mask:0xf bank_mask:0x8\n\t"
"v_mfma_f32_16x16x32_bf16 a[72:75], a[128:131], v[184:187], a[72:75]\n\t"
"ds_read_b64_tr_b16 a[196:197], v13 offset:24576\n\t"
"v_mfma_f32_16x16x32_bf16 a[72:75], a[144:147], v[196:199], a[72:75]\n\t"
"v_mfma_f32_16x16x32_bf16 a[76:79], a[148:151], v[196:199], a[76:79]\n\t"
"ds_read_b64_tr_b16 a[198:199], v13 offset:25600\n\t"
"v_mfma_f32_16x16x32_bf16 a[76:79], a[132:135], v[184:187], a[76:79]\n\t"
"v_mov_b32_dpp v227, v115 row_shr:12 row_mask:0xf bank_mask:0x8\n\t"
"v_mfma_f32_16x16x32_bf16 a[80:83], a[136:139], v[184:187], a[80:83]\n\t"
"ds_read_b64_tr_b16 a[212:213], v13 offset:28672\n\t"
"v_mfma_f32_16x16x32_bf16 a[80:83], a[152:155], v[196:199], a[80:83]\n\t"
"v_mfma_f32_16x16x32_bf16 v[88:91], a[156:159], v[196:199], v[88:91]\n\t"
"ds_read_b64_tr_b16 a[214:215], v13 offset:29696\n\t"
"v_mfma_f32_16x16x32_bf16 v[88:91], a[140:143], v[184:187], v[88:91]\n\t"
"v_mov_b32_dpp v224, v148 row_shl:1 row_mask:0xf bank_mask:0xf\n\t"
"v_mfma_f32_16x16x32_bf16 v[92:95], a[140:143], v[200:203], v[92:95]\n\t"
"ds_read_b64_tr_b16 a[228:229], v13 offset:32768\n\t"
"v_mfma_f32_16x16x32_bf16 v[92:95], a[156:159], v[208:211], v[92:95]\n\t"
"v_mfma_f32_16x16x32_bf16 a[92:95], a[152:155], v[208:211], a[92:95]\n\t"
"ds_read_b64_tr_b16 a[230:231], v13 offset:33792\n\t"
"v_mov_b32_dpp v225, v149 row_shl:1 row_mask:0xf bank_mask:0xf\n\t"
"v_mfma_f32_16x16x32_bf16 a[92:95], a[136:139], v[200:203], a[92:95]\n\t"
"ds_read_b64_tr_b16 a[244:245], v13 offset:36864\n\t"
"v_mov_b32_dpp v226, v150 row_shl:1 row_mask:0xf bank_mask:0xf\n\t"
"v_mfma_f32_16x16x32_bf16 a[88:91], a[132:135], v[200:203], a[88:91]\n\t"
"ds_read_b64_tr_b16 a[246:247], v13 offset:37888\n\t"
"v_mov_b32_dpp v227, v151 row_shl:1 row_mask:0xf bank_mask:0xf\n\t"
"v_mfma_f32_16x16x32_bf16 a[88:91], a[148:151], v[208:211], a[88:91]\n\t"
"v_mfma_f32_16x16x32_bf16 a[84:87], a[144:147], v[208:211], a[84:87]\n\t"
"v_mfma_f32_16x16x32_bf16 a[84:87], a[128:131], v[200:203], a[84:87]\n\t"
"ds_read_b64_tr_b16 a[168:169], v14 offset:16384\n\t"
"ds_read_b64_tr_b16 a[170:171], v14 offset:17408\n\t"
"s_waitcnt lgkmcnt(15)\n\t"
"v_mfma_f32_16x16x32_bf16 a[0:3], a[160:163], v[156:159], a[0:3]\n\t"
"ds_read_b64_tr_b16 a[184:185], v14 offset:20480\n\t"
"ds_read_b64_tr_b16 a[186:187], v14 offset:21504\n\t"
"v_mfma_f32_16x16x32_bf16 a[0:3], a[176:179], v[132:135], a[0:3]\n\t"
"ds_read_b64_tr_b16 a[200:201], v14 offset:24576\n\t"
"ds_read_b64_tr_b16 a[202:203], v14 offset:25600\n\t"
"v_mfma_f32_16x16x32_bf16 a[0:3], a[192:195], v[140:143], a[0:3]\n\t"
"ds_read_b64_tr_b16 a[216:217], v14 offset:28672\n\t"
"ds_read_b64_tr_b16 a[218:219], v14 offset:29696\n\t"
"v_mfma_f32_16x16x32_bf16 a[0:3], a[208:211], v[164:167], a[0:3]\n\t"
"ds_read_b64_tr_b16 a[232:233], v14 offset:32768\n\t"
"ds_read_b64_tr_b16 a[234:235], v14 offset:33792\n\t"
"v_mfma_f32_16x16x32_bf16 a[0:3], a[224:227], v[244:247], a[0:3]\n\t"
"ds_read_b64_tr_b16 a[248:249], v14 offset:36864\n\t"
"ds_read_b64_tr_b16 a[250:251], v14 offset:37888\n\t"
"s_waitcnt lgkmcnt(15)\n\t"
"v_mfma_f32_16x16x32_bf16 a[0:3], a[240:243], v[188:191], a[0:3]\n\t"
"ds_read_b64_tr_b16 a[172:173], v15 offset:16384\n\t"
"ds_read_b64_tr_b16 a[174:175], v15 offset:17408\n\t"
"v_mfma_f32_16x16x32_bf16 a[12:15], a[240:243], v[204:207], a[12:15]\n\t"
"ds_read_b64_tr_b16 a[188:189], v15 offset:20480\n\t"
"ds_read_b64_tr_b16 a[190:191], v15 offset:21504\n\t"
"v_mfma_f32_16x16x32_bf16 a[12:15], a[224:227], v[192:195], a[12:15]\n\t"
"ds_read_b64_tr_b16 a[204:205], v15 offset:24576\n\t"
"ds_read_b64_tr_b16 a[206:207], v15 offset:25600\n\t"
"v_mfma_f32_16x16x32_bf16 a[12:15], a[208:211], v[188:191], a[12:15]\n\t"
"ds_read_b64_tr_b16 a[220:221], v15 offset:28672\n\t"
"ds_read_b64_tr_b16 a[222:223], v15 offset:29696\n\t"
"v_mfma_f32_16x16x32_bf16 a[12:15], a[192:195], v[244:247], a[12:15]\n\t"
"ds_read_b64_tr_b16 a[236:237], v15 offset:32768\n\t"
"ds_read_b64_tr_b16 a[238:239], v15 offset:33792\n\t"
"v_mfma_f32_16x16x32_bf16 a[12:15], a[176:179], v[164:167], a[12:15]\n\t"
"ds_read_b64_tr_b16 a[252:253], v15 offset:36864\n\t"
"ds_read_b64_tr_b16 a[254:255], v15 offset:37888\n\t"
"v_mfma_f32_16x16x32_bf16 a[12:15], a[160:163], v[140:143], a[12:15]\n\t"
"ds_write_b128 v18, a[0:3] offset:0\n\t"
"v_mfma_f32_16x16x32_bf16 a[24:27], a[160:163], v[244:247], a[24:27]\n\t"
"v_mov_b32_dpp v228, v108 row_shr:11 row_mask:0xf bank_mask:0x8\n\t"
"v_mov_b32_dpp v229, v109 row_shr:11 row_mask:0xf bank_mask:0x8\n\t"
"v_mfma_f32_16x16x32_bf16 a[24:27], a[176:179], v[188:191], a[24:27]\n\t"
"v_mov_b32_dpp v230, v110 row_shr:11 row_mask:0xf bank_mask:0x8\n\t"
"v_mov_b32_dpp v231, v111 row_shr:11 row_mask:0xf bank_mask:0x8\n\t"
"v_mfma_f32_16x16x32_bf16 a[24:27], a[192:195], v[192:195], a[24:27]\n\t"
"v_mov_b32_dpp v228, v156 row_shl:1 row_mask:0xf bank_mask:0xf\n\t"
"v_mov_b32_dpp v229, v157 row_shl:1 row_mask:0xf bank_mask:0xf\n\t"
"v_mfma_f32_16x16x32_bf16 a[24:27], a[208:211], v[204:207], a[24:27]\n\t"
"v_mov_b32_dpp v230, v158 row_shl:1 row_mask:0xf bank_mask:0xf\n\t"
"v_mov_b32_dpp v231, v159 row_shl:1 row_mask:0xf bank_mask:0xf\n\t"
"v_mfma_f32_16x16x32_bf16 a[24:27], a[224:227], v[212:215], a[24:27]\n\t"
"v_mov_b32_dpp v236, v112 row_shr:11 row_mask:0xf bank_mask:0x8\n\t"
"v_mov_b32_dpp v237, v113 row_shr:11 row_mask:0xf bank_mask:0x8\n\t"
"v_mfma_f32_16x16x32_bf16 a[24:27], a[240:243], v[176:179], a[24:27]\n\t"
"ds_write_b128 v18, a[12:15] offset:1024\n\t"
"s_waitcnt lgkmcnt(15)\n\t"
"v_mfma_f32_16x16x32_bf16 a[4:7], a[244:247], v[188:191], a[4:7]\n\t"
"v_mov_b32_dpp v238, v114 row_shr:11 row_mask:0xf bank_mask:0x8\n\t"
"v_mov_b32_dpp v239, v115 row_shr:11 row_mask:0xf bank_mask:0x8\n\t"
"v_mfma_f32_16x16x32_bf16 a[4:7], a[228:231], v[244:247], a[4:7]\n\t"
"v_mov_b32_dpp v236, v132 row_shl:1 row_mask:0xf bank_mask:0xf\n\t"
"v_mov_b32_dpp v237, v133 row_shl:1 row_mask:0xf bank_mask:0xf\n\t"
"v_mfma_f32_16x16x32_bf16 a[4:7], a[212:215], v[164:167], a[4:7]\n\t"
"v_mov_b32_dpp v238, v134 row_shl:1 row_mask:0xf bank_mask:0xf\n\t"
"v_mov_b32_dpp v239, v135 row_shl:1 row_mask:0xf bank_mask:0xf\n\t"
"v_mfma_f32_16x16x32_bf16 a[4:7], a[196:199], v[140:143], a[4:7]\n\t"
"v_mov_b32_dpp v248, v108 row_shr:10 row_mask:0xf bank_mask:0x8\n\t"
"v_mov_b32_dpp v249, v109 row_shr:10 row_mask:0xf bank_mask:0x8\n\t"
"v_mfma_f32_16x16x32_bf16 a[4:7], a[180:183], v[132:135], a[4:7]\n\t"
"v_mov_b32_dpp v250, v110 row_shr:10 row_mask:0xf bank_mask:0x8\n\t"
"v_mov_b32_dpp v251, v111 row_shr:10 row_mask:0xf bank_mask:0x8\n\t"
"v_mfma_f32_16x16x32_bf16 a[4:7], a[164:167], v[156:159], a[4:7]\n\t"
"ds_write_b128 v18, a[24:27] offset:2048\n\t"
"v_mfma_f32_16x16x32_bf16 a[16:19], a[164:167], v[140:143], a[16:19]\n\t"
"v_mov_b32_dpp v248, v140 row_shl:1 row_mask:0xf bank_mask:0xf\n\t"
"v_mov_b32_dpp v249, v141 row_shl:1 row_mask:0xf bank_mask:0xf\n\t"
"v_mfma_f32_16x16x32_bf16 a[16:19], a[180:183], v[164:167], a[16:19]\n\t"
"v_mov_b32_dpp v250, v142 row_shl:1 row_mask:0xf bank_mask:0xf\n\t"
"v_mov_b32_dpp v251, v143 row_shl:1 row_mask:0xf bank_mask:0xf\n\t"
"v_mfma_f32_16x16x32_bf16 a[16:19], a[196:199], v[244:247], a[16:19]\n\t"
"v_mov_b32_dpp v120, v112 row_shr:10 row_mask:0xf bank_mask:0x8\n\t"
"v_mov_b32_dpp v121, v113 row_shr:10 row_mask:0xf bank_mask:0x8\n\t"
"v_mfma_f32_16x16x32_bf16 a[16:19], a[212:215], v[188:191], a[16:19]\n\t"
"v_mov_b32_dpp v122, v114 row_shr:10 row_mask:0xf bank_mask:0x8\n\t"
"v_mov_b32_dpp v123, v115 row_shr:10 row_mask:0xf bank_mask:0x8\n\t"
"v_mfma_f32_16x16x32_bf16 a[16:19], a[228:231], v[192:195], a[16:19]\n\t"
"v_mov_b32_dpp v120, v164 row_shl:1 row_mask:0xf bank_mask:0xf\n\t"
"v_mov_b32_dpp v121, v165 row_shl:1 row_mask:0xf bank_mask:0xf\n\t"
"v_mfma_f32_16x16x32_bf16 a[16:19], a[244:247], v[204:207], a[16:19]\n\t"
"ds_write_b128 v18, a[4:7] offset:3072\n\t"
"v_mfma_f32_16x16x32_bf16 a[28:31], a[244:247], v[176:179], a[28:31]\n\t"
"v_mov_b32_dpp v122, v166 row_shl:1 row_mask:0xf bank_mask:0xf\n\t"
"v_mov_b32_dpp v123, v167 row_shl:1 row_mask:0xf bank_mask:0xf\n\t"
"v_mfma_f32_16x16x32_bf16 a[28:31], a[228:231], v[212:215], a[28:31]\n\t"
"v_add_u32 v97, 0x9e3779b9, v26\n\t"
"v_add_u32 v99, 0x9e3779b9, v26\n\t"
"v_mfma_f32_16x16x32_bf16 a[28:31], a[212:215], v[204:207], a[28:31]\n\t"
"v_add_u32 v26, 0x9e3779b9, v26\n\t"
"v_mfma_f32_16x16x32_bf16 a[28:31], a[196:199], v[192:195], a[28:31]\n\t"
"v_mfma_f32_16x16x32_bf16 a[28:31], a[180:183], v[188:191], a[28:31]\n\t"
"v_mfma_f32_16x16x32_bf16 a[28:31], a[164:167], v[244:247], a[28:31]\n\t"
"ds_write_b128 v18, a[16:19] offset:4096\n\t"
"v_mfma_f32_16x16x32_bf16 a[8:11], a[168:171], v[156:159], a[8:11]\n\t"
"v_mfma_f32_16x16x32_bf16 a[8:11], a[184:187], v[132:135], a[8:11]\n\t"
"v_mfma_f32_16x16x32_bf16 a[8:11], a[200:203], v[140:143], a[8:11]\n\t"
"v_mfma_f32_16x16x32_bf16 a[8:11], a[216:219], v[164:167], a[8:11]\n\t"
"v_mfma_f32_16x16x32_bf16 a[8:11], a[232:235], v[244:247], a[8:11]\n\t"
"s_waitcnt lgkmcnt(15)\n\t"
"v_mfma_f32_16x16x32_bf16 a[8:11], a[248:251], v[188:191], a[8:11]\n\t"
"ds_write_b128 v18, a[28:31] offset:5120\n\t"
"v_mfma_f32_16x16x32_bf16 a[20:23], a[248:251], v[204:207], a[20:23]\n\t"
"v_mfma_f32_16x16x32_bf16 a[20:23], a[232:235], v[192:195], a[20:23]\n\t"
"v_mfma_f32_16x16x32_bf16 a[20:23], a[216:219], v[188:191], a[20:23]\n\t"
"v_mfma_f32_16x16x32_bf16 a[20:23], a[200:203], v[244:247], a[20:23]\n\t"
"v_mfma_f32_16x16x32_bf16 a[20:23], a[184:187], v[164:167], a[20:23]\n\t"
"v_mfma_f32_16x16x32_bf16 a[20:23], a[168:171], v[140:143], a[20:23]\n\t"
"v_mfma_f32_16x16x32_bf16 a[32:35], a[168:171], v[244:247], a[32:35]\n\t"
"v_mfma_f32_16x16x32_bf16 a[32:35], a[184:187], v[188:191], a[32:35]\n\t"
"v_mfma_f32_16x16x32_bf16 a[32:35], a[200:203], v[192:195], a[32:35]\n\t"
"v_mfma_f32_16x16x32_bf16 a[32:35], a[216:219], v[204:207], a[32:35]\n\t"
"v_mfma_f32_16x16x32_bf16 a[32:35], a[232:235], v[212:215], a[32:35]\n\t"
"v_mfma_f32_16x16x32_bf16 a[32:35], a[248:251], v[176:179], a[32:35]\n\t"
"ds_write_b128 v18, a[8:11] offset:6144\n\t"
"v_mfma_f32_16x16x32_bf16 a[36:39], a[240:243], v[196:199], a[36:39]\n\t"
"v_mfma_f32_16x16x32_bf16 a[36:39], a[224:227], v[184:187], a[36:39]\n\t"
"v_mfma_f32_16x16x32_bf16 a[36:39], a[208:211], v[176:179], a[36:39]\n\t"
"v_mfma_f32_16x16x32_bf16 a[36:39], a[192:195], v[212:215], a[36:39]\n\t"
"v_mfma_f32_16x16x32_bf16 a[36:39], a[176:179], v[204:207], a[36:39]\n\t"
"v_mfma_f32_16x16x32_bf16 a[36:39], a[160:163], v[192:195], a[36:39]\n\t"
"ds_write_b128 v18, a[20:23] offset:7168\n\t"
"v_mfma_f32_16x16x32_bf16 a[48:51], a[160:163], v[212:215], a[48:51]\n\t"
"v_mfma_f32_16x16x32_bf16 a[48:51], a[176:179], v[176:179], a[48:51]\n\t"
"v_mfma_f32_16x16x32_bf16 a[48:51], a[192:195], v[184:187], a[48:51]\n\t"
"v_mfma_f32_16x16x32_bf16 a[48:51], a[208:211], v[196:199], a[48:51]\n\t"
"v_mfma_f32_16x16x32_bf16 a[48:51], a[224:227], v[200:203], a[48:51]\n\t"
"v_mfma_f32_16x16x32_bf16 a[48:51], a[240:243], v[208:211], a[48:51]\n\t"
"ds_write_b128 v18, a[32:35] offset:8192\n\t"
"v_mfma_f32_16x16x32_bf16 a[60:63], a[240:243], v[224:227], a[60:63]\n\t"
"v_mfma_f32_16x16x32_bf16 a[60:63], a[224:227], v[216:219], a[60:63]\n\t"
"v_mfma_f32_16x16x32_bf16 a[60:63], a[208:211], v[208:211], a[60:63]\n\t"
"v_mfma_f32_16x16x32_bf16 a[60:63], a[192:195], v[200:203], a[60:63]\n\t"
"s_waitcnt lgkmcnt(0)\n\t"
"s_barrier\n\t"
"ds_read_b128 v[172:175], v19 offset:0\n\t"
"v_mfma_f32_16x16x32_bf16 a[60:63], a[176:179], v[196:199], a[60:63]\n\t"
"ds_read_b128 v[232:235], v20 offset:3072\n\t"
"v_mfma_f32_16x16x32_bf16 a[60:63], a[160:163], v[184:187], a[60:63]\n\t"
"ds_write_b128 v18, a[36:39] offset:9216\n\t"
"v_mfma_f32_16x16x32_bf16 a[40:43], a[164:167], v[192:195], a[40:43]\n\t"
"v_mfma_f32_16x16x32_bf16 a[40:43], a[180:183], v[204:207], a[40:43]\n\t"
"ds_read_b128 v[240:243], v21 offset:6144\n\t"
"v_mfma_f32_16x16x32_bf16 a[40:43], a[196:199], v[212:215], a[40:43]\n\t"
"ds_read_b128 v[252:255], v19 offset:1024\n\t"
"v_mfma_f32_16x16x32_bf16 a[40:43], a[212:215], v[176:179], a[40:43]\n\t"
"ds_read_b128 v[116:119], v20 offset:4096\n\t"
"v_mfma_f32_16x16x32_bf16 a[40:43], a[228:231], v[184:187], a[40:43]\n\t"
"ds_read_b128 v[144:147], v21 offset:7168\n\t"
"v_mfma_f32_16x16x32_bf16 a[40:43], a[244:247], v[196:199], a[40:43]\n\t"
"ds_write_b128 v18, a[48:51] offset:10240\n\t"
"v_mfma_f32_16x16x32_bf16 a[52:55], a[244:247], v[208:211], a[52:55]\n\t"
"s_waitcnt lgkmcnt(6)\n\t"
"v_add_f32 v172, v172, v232\n\t"
"v_add_f32 v173, v173, v233\n\t"
"v_mfma_f32_16x16x32_bf16 a[52:55], a[228:231], v[200:203], a[52:55]\n\t"
"ds_read_b128 v[220:223], v19 offset:2048\n\t"
"v_add_f32 v174, v174, v234\n\t"
"v_mfma_f32_16x16x32_bf16 a[52:55], a[212:215], v[196:199], a[52:55]\n\t"
"ds_read_b128 v[124:127], v20 offset:5120\n\t"
"v_add_f32 v175, v175, v235\n\t"
"v_mfma_f32_16x16x32_bf16 a[52:55], a[196:199], v[184:187], a[52:55]\n\t"
"ds_read_b128 v[152:155], v21 offset:8192\n\t"
"s_waitcnt lgkmcnt(7)\n\t"
"v_add_f32 v172, v172, v240\n\t"
"v_mfma_f32_16x16x32_bf16 a[52:55], a[180:183], v[176:179], a[52:55]\n\t"
"v_add_f32 v173, v173, v241\n\t"
"v_add_f32 v174, v174, v242\n\t"
"v_mfma_f32_16x16x32_bf16 a[52:55], a[164:167], v[212:215], a[52:55]\n\t"
"ds_write_b128 v18, a[60:63] offset:11264\n\t"
"v_mfma_f32_16x16x32_bf16 a[64:67], a[164:167], v[184:187], a[64:67]\n\t"
"v_add_f32 v175, v175, v243\n\t"
"v_add_f32 v64, v64, v172\n\t"
"v_mfma_f32_16x16x32_bf16 a[64:67], a[180:183], v[196:199], a[64:67]\n\t"
"v_add_f32 v65, v65, v173\n\t"
"v_add_f32 v66, v66, v174\n\t"
"v_mfma_f32_16x16x32_bf16 a[64:67], a[196:199], v[200:203], a[64:67]\n\t"
"v_add_f32 v67, v67, v175\n\t"
"s_waitcnt lgkmcnt(6)\n\t"
"v_add_f32 v252, v252, v116\n\t"
"v_mfma_f32_16x16x32_bf16 a[64:67], a[212:215], v[208:211], a[64:67]\n\t"
"v_add_f32 v253, v253, v117\n\t"
"v_add_f32 v254, v254, v118\n\t"
"v_mfma_f32_16x16x32_bf16 a[64:67], a[228:231], v[216:219], a[64:67]\n\t"
"v_add_f32 v255, v255, v119\n\t"
"s_waitcnt lgkmcnt(5)\n\t"
"v_add_f32 v252, v252, v144\n\t"
"v_mfma_f32_16x16x32_bf16 a[64:67], a[244:247], v[224:227], a[64:67]\n\t"
"ds_write_b128 v18, a[40:43] offset:12288\n\t"
"v_mfma_f32_16x16x32_bf16 v[64:67], a[252:255], v[188:191], v[64:67]\n\t"
"v_add_f32 v253, v253, v145\n\t"
"v_add_f32 v254, v254, v146\n\t"
"v_mfma_f32_16x16x32_bf16 v[64:67], a[236:239], v[244:247], v[64:67]\n\t"
"v_add_f32 v255, v255, v147\n\t"
"v_add_f32 v68, v68, v252\n\t"
"v_mfma_f32_16x16x32_bf16 v[64:67], a[220:223], v[164:167], v[64:67]\n\t"
"v_add_f32 v69, v69, v253\n\t"
"v_add_f32 v70, v70, v254\n\t"
"v_mfma_f32_16x16x32_bf16 v[64:67], a[204:207], v[140:143], v[64:67]\n\t"
"v_add_f32 v71, v71, v255\n\t"
"s_waitcnt lgkmcnt(3)\n\t"
"v_add_f32 v220, v220, v124\n\t"
"v_mfma_f32_16x16x32_bf16 v[64:67], a[188:191], v[132:135], v[64:67]\n\t"
"v_add_f32 v221, v221, v125\n\t"
"v_add_f32 v222, v222, v126\n\t"
"v_mfma_f32_16x16x32_bf16 v[64:67], a[172:175], v[156:159], v[64:67]\n\t"
"ds_write_b128 v18, a[52:55] offset:13312\n\t"
"v_mfma_f32_16x16x32_bf16 a[44:47], a[168:171], v[192:195], a[44:47]\n\t"
"v_add_f32 v223, v223, v127\n\t"
"s_waitcnt lgkmcnt(3)\n\t"
"v_add_f32 v220, v220, v152\n\t"
"v_mfma_f32_16x16x32_bf16 a[44:47], a[184:187], v[204:207], a[44:47]\n\t"
"v_add_f32 v221, v221, v153\n\t"
"v_add_f32 v222, v222, v154\n\t"
"v_mfma_f32_16x16x32_bf16 a[44:47], a[200:203], v[212:215], a[44:47]\n\t"
"v_add_f32 v223, v223, v155\n\t"
"v_add_f32 v72, v72, v220\n\t"
"v_mfma_f32_16x16x32_bf16 a[44:47], a[216:219], v[176:179], a[44:47]\n\t"
"v_add_f32 v73, v73, v221\n\t"
"v_add_f32 v74, v74, v222\n\t"
"v_mfma_f32_16x16x32_bf16 a[44:47], a[232:235], v[184:187], a[44:47]\n\t"
"v_add_f32 v75, v75, v223\n\t"
"v_mov_b32_dpp v40, v64 row_newbcast:0 row_mask:0xf bank_mask:0xf\n\t"
"v_mfma_f32_16x16x32_bf16 a[44:47], a[248:251], v[196:199], a[44:47]\n\t"
"ds_write_b128 v18, a[64:67] offset:14336\n\t"
"v_mfma_f32_16x16x32_bf16 a[56:59], a[248:251], v[208:211], a[56:59]\n\t"
"v_mov_b32_dpp v41, v65 row_newbcast:0 row_mask:0xf bank_mask:0xf\n\t"
"v_mov_b32_dpp v42, v66 row_newbcast:0 row_mask:0xf bank_mask:0xf\n\t"
"v_mfma_f32_16x16x32_bf16 a[56:59], a[232:235], v[200:203], a[56:59]\n\t"
"v_mov_b32_dpp v43, v67 row_newbcast:0 row_mask:0xf bank_mask:0xf\n\t"
"v_mov_b32_dpp v39, v64 row_newbcast:0 row_mask:0xf bank_mask:1\n\t"
"v_mfma_f32_16x16x32_bf16 a[56:59], a[216:219], v[196:199], a[56:59]\n\t"
"v_mov_b32_dpp v39, v65 row_newbcast:0 row_mask:0xf bank_mask:2\n\t"
"v_mov_b32_dpp v39, v66 row_newbcast:0 row_mask:0xf bank_mask:4\n\t"
"v_mfma_f32_16x16x32_bf16 a[56:59], a[200:203], v[184:187], a[56:59]\n\t"
"v_mov_b32_dpp v39, v67 row_newbcast:0 row_mask:0xf bank_mask:8\n\t"
"v_sub_f32 v68, v68, v40\n\t"
"v_mfma_f32_16x16x32_bf16 a[56:59], a[184:187], v[176:179], a[56:59]\n\t"
"v_sub_f32 v69, v69, v41\n\t"
"v_sub_f32 v70, v70, v42\n\t"
"v_mfma_f32_16x16x32_bf16 a[56:59], a[168:171], v[212:215], a[56:59]\n\t"
"ds_write_b128 v18, a[44:47] offset:15360\n\t"
"v_mfma_f32_16x16x32_bf16 a[68:71], a[168:171], v[184:187], a[68:71]\n\t"
"v_sub_f32 v71, v71, v43\n\t"
"v_sub_f32 v72, v72, v40\n\t"
"v_mfma_f32_16x16x32_bf16 a[68:71], a[184:187], v[196:199], a[68:71]\n\t"
"v_sub_f32 v73, v73, v41\n\t"
"v_sub_f32 v74, v74, v42\n\t"
"v_mfma_f32_16x16x32_bf16 a[68:71], a[200:203], v[200:203], a[68:71]\n\t"
"v_sub_f32 v75, v75, v43\n\t"
"v_sub_f32 v76, v76, v40\n\t"
"v_mfma_f32_16x16x32_bf16 a[68:71], a[216:219], v[208:211], a[68:71]\n\t"
"v_sub_f32 v77, v77, v41\n\t"
"v_sub_f32 v78, v78, v42\n\t"
"v_mfma_f32_16x16x32_bf16 a[68:71], a[232:235], v[216:219], a[68:71]\n\t"
"v_sub_f32 v79, v79, v43\n\t"
"v_sub_f32 v80, v80, v40\n\t"
"v_mfma_f32_16x16x32_bf16 a[68:71], a[248:251], v[224:227], a[68:71]\n\t"
"ds_write_b128 v18, a[56:59] offset:16384\n\t"
"v_mfma_f32_16x16x32_bf16 a[72:75], a[240:243], v[236:239], a[72:75]\n\t"
"v_sub_f32 v81, v81, v41\n\t"
"v_sub_f32 v82, v82, v42\n\t"
"v_mfma_f32_16x16x32_bf16 a[72:75], a[224:227], v[228:231], a[72:75]\n\t"
"v_sub_f32 v83, v83, v43\n\t"
"v_sub_f32 v84, v84, v40\n\t"
"v_mfma_f32_16x16x32_bf16 a[72:75], a[208:211], v[224:227], a[72:75]\n\t"
"v_sub_f32 v85, v85, v41\n\t"
"v_sub_f32 v86, v86, v42\n\t"
"v_mfma_f32_16x16x32_bf16 a[72:75], a[192:195], v[216:219], a[72:75]\n\t"
"v_sub_f32 v87, v87, v43\n\t"
"v_sub_f32 v88, v88, v40\n\t"
"v_mfma_f32_16x16x32_bf16 a[72:75], a[176:179], v[208:211], a[72:75]\n\t"
"v_sub_f32 v89, v89, v41\n\t"
"v_sub_f32 v90, v90, v42\n\t"
"v_mfma_f32_16x16x32_bf16 a[72:75], a[160:163], v[200:203], a[72:75]\n\t"
"ds_write_b128 v18, a[68:71] offset:17408\n\t"
"v_mfma_f32_16x16x32_bf16 a[84:87], a[160:163], v[216:219], a[84:87]\n\t"
"v_sub_f32 v91, v91, v43\n\t"
"v_sub_f32 v92, v92, v40\n\t"
"v_mfma_f32_16x16x32_bf16 a[84:87], a[176:179], v[224:227], a[84:87]\n\t"
"v_sub_f32 v93, v93, v41\n\t"
"v_sub_f32 v94, v94, v42\n\t"
"v_mfma_f32_16x16x32_bf16 a[84:87], a[192:195], v[228:231], a[84:87]\n\t"
"v_sub_f32 v95, v95, v43\n\t"
"v_sub_f32 v64, v64, v40\n\t"
"v_mfma_f32_16x16x32_bf16 a[84:87], a[208:211], v[236:239], a[84:87]\n\t"
"s_waitcnt lgkmcnt(0)\n\t"
"s_barrier\n\t"
"ds_read_b128 v[160:163], v19 offset:9216\n\t"
"v_mfma_f32_16x16x32_bf16 a[84:87], a[224:227], v[248:251], a[84:87]\n\t"
"ds_read_b128 v[136:139], v20 offset:12288\n\t"
"v_cndmask_b32 v32, 0, v64, s[28:29]\n\t"
"v_mfma_f32_16x16x32_bf16 a[84:87], a[240:243], v[120:123], a[84:87]\n\t"
"ds_write_b128 v18, a[72:75] offset:18432\n\t"
"v_mfma_f32_16x16x32_bf16 a[76:79], a[244:247], v[236:239], a[76:79]\n\t"
"v_mul_f32 v58, v32, v32\n\t"
"v_sub_f32 v65, v65, v41\n\t"
"v_mfma_f32_16x16x32_bf16 a[76:79], a[228:231], v[228:231], a[76:79]\n\t"
"ds_read_b128 v[168:171], v21 offset:15360\n\t"
"v_cndmask_b32 v33, 0, v65, s[28:29]\n\t"
"v_mfma_f32_16x16x32_bf16 a[76:79], a[212:215], v[224:227], a[76:79]\n\t"
"ds_read_b128 v[180:183], v19 offset:10240\n\t"
"v_mul_f32 v59, v33, v33\n\t"
"v_mfma_f32_16x16x32_bf16 a[76:79], a[196:199], v[216:219], a[76:79]\n\t"
"ds_read_b128 v[128:131], v20 offset:13312\n\t"
"v_sub_f32 v66, v66, v42\n\t"
"v_mfma_f32_16x16x32_bf16 a[76:79], a[180:183], v[208:211], a[76:79]\n\t"
"ds_read_b128 v[148:151], v21 offset:16384\n\t"
"v_cndmask_b32 v34, 0, v66, s[28:29]\n\t"
"v_mfma_f32_16x16x32_bf16 a[76:79], a[164:167], v[200:203], a[76:79]\n\t"
"ds_write_b128 v18, a[84:87] offset:19456\n\t"
"v_mfma_f32_16x16x32_bf16 a[88:91], a[164:167], v[216:219], a[88:91]\n\t"
"s_waitcnt lgkmcnt(6)\n\t"
"v_add_f32 v160, v160, v136\n\t"
"v_add_f32 v161, v161, v137\n\t"
"v_mfma_f32_16x16x32_bf16 a[88:91], a[180:183], v[224:227], a[88:91]\n\t"
"ds_read_b128 v[232:235], v19 offset:11264\n\t"
"v_add_f32 v162, v162, v138\n\t"
"v_mfma_f32_16x16x32_bf16 a[88:91], a[196:199], v[228:231], a[88:91]\n\t"
"ds_read_b128 v[240:243], v20 offset:14336\n\t"
"v_add_f32 v163, v163, v139\n\t"
"v_mfma_f32_16x16x32_bf16 a[88:91], a[212:215], v[236:239], a[88:91]\n\t"
"ds_read_b128 v[172:175], v21 offset:17408\n\t"
"s_waitcnt lgkmcnt(7)\n\t"
"v_add_f32 v160, v160, v168\n\t"
"v_mfma_f32_16x16x32_bf16 a[88:91], a[228:231], v[248:251], a[88:91]\n\t"
"v_add_f32 v161, v161, v169\n\t"
"v_add_f32 v162, v162, v170\n\t"
"v_mfma_f32_16x16x32_bf16 a[88:91], a[244:247], v[120:123], a[88:91]\n\t"
"ds_write_b128 v18, a[76:79] offset:20480\n\t"
"v_mfma_f32_16x16x32_bf16 a[80:83], a[248:251], v[236:239], a[80:83]\n\t"
"v_add_f32 v163, v163, v171\n\t"
"v_add_f32 v76, v76, v160\n\t"
"v_mfma_f32_16x16x32_bf16 a[80:83], a[232:235], v[228:231], a[80:83]\n\t"
"v_add_f32 v77, v77, v161\n\t"
"v_add_f32 v78, v78, v162\n\t"
"v_mfma_f32_16x16x32_bf16 a[80:83], a[216:219], v[224:227], a[80:83]\n\t"
"v_add_f32 v79, v79, v163\n\t"
"s_waitcnt lgkmcnt(6)\n\t"
"v_add_f32 v180, v180, v128\n\t"
"v_mfma_f32_16x16x32_bf16 a[80:83], a[200:203], v[216:219], a[80:83]\n\t"
"v_add_f32 v181, v181, v129\n\t"
"v_add_f32 v182, v182, v130\n\t"
"v_mfma_f32_16x16x32_bf16 a[80:83], a[184:187], v[208:211], a[80:83]\n\t"
"v_add_f32 v183, v183, v131\n\t"
"s_waitcnt lgkmcnt(5)\n\t"
"v_add_f32 v180, v180, v148\n\t"
"v_mfma_f32_16x16x32_bf16 a[80:83], a[168:171], v[200:203], a[80:83]\n\t"
"ds_write_b128 v18, a[88:91] offset:21504\n\t"
"v_mfma_f32_16x16x32_bf16 a[92:95], a[168:171], v[216:219], a[92:95]\n\t"
"v_add_f32 v181, v181, v149\n\t"
"v_add_f32 v182, v182, v150\n\t"
"v_mfma_f32_16x16x32_bf16 a[92:95], a[184:187], v[224:227], a[92:95]\n\t"
"v_add_f32 v183, v183, v151\n\t"
"v_add_f32 v80, v80, v180\n\t"
"v_mfma_f32_16x16x32_bf16 a[92:95], a[200:203], v[228:231], a[92:95]\n\t"
"v_add_f32 v81, v81, v181\n\t"
"v_add_f32 v82, v82, v182\n\t"
"v_mfma_f32_16x16x32_bf16 a[92:95], a[216:219], v[236:239], a[92:95]\n\t"
"v_add_f32 v83, v83, v183\n\t"
"s_waitcnt lgkmcnt(3)\n\t"
"v_add_f32 v232, v232, v240\n\t"
"v_mfma_f32_16x16x32_bf16 a[92:95], a[232:235], v[248:251], a[92:95]\n\t"
"v_add_f32 v233, v233, v241\n\t"
"v_add_f32 v234, v234, v242\n\t"
"v_mfma_f32_16x16x32_bf16 a[92:95], a[248:251], v[120:123], a[92:95]\n\t"
"ds_write_b128 v18, a[80:83] offset:22528\n\t"
"v_mfma_f32_16x16x32_bf16 v[68:71], a[252:255], v[204:207], v[68:71]\n\t"
"v_add_f32 v235, v235, v243\n\t"
"s_waitcnt lgkmcnt(3)\n\t"
"v_add_f32 v232, v232, v172\n\t"
"v_mfma_f32_16x16x32_bf16 v[68:71], a[236:239], v[192:195], v[68:71]\n\t"
"v_add_f32 v233, v233, v173\n\t"
"v_add_f32 v234, v234, v174\n\t"
"v_mfma_f32_16x16x32_bf16 v[68:71], a[220:223], v[188:191], v[68:71]\n\t"
"v_add_f32 v235, v235, v175\n\t"
"v_add_f32 v84, v84, v232\n\t"
"v_mfma_f32_16x16x32_bf16 v[68:71], a[204:207], v[244:247], v[68:71]\n\t"
"v_add_f32 v85, v85, v233\n\t"
"v_add_f32 v86, v86, v234\n\t"
"v_mfma_f32_16x16x32_bf16 v[68:71], a[188:191], v[164:167], v[68:71]\n\t"
"v_add_f32 v87, v87, v235\n\t"
"v_mul_f32 v60, v34, v34\n\t"
"v_mfma_f32_16x16x32_bf16 v[68:71], a[172:175], v[140:143], v[68:71]\n\t"
"ds_write_b128 v18, a[92:95] offset:23552\n\t"
"v_mfma_f32_16x16x32_bf16 v[72:75], a[172:175], v[244:247], v[72:75]\n\t"
"v_sub_f32 v67, v67, v43\n\t"
"v_mov_b32 v44, v64\n\t"
"v_mfma_f32_16x16x32_bf16 v[72:75], a[188:191], v[188:191], v[72:75]\n\t"
"v_cndmask_b32 v35, 0, v67, s[28:29]\n\t"
"v_mul_f32 v61, v35, v35\n\t"
"v_mfma_f32_16x16x32_bf16 v[72:75], a[204:207], v[192:195], v[72:75]\n\t"
"v_mov_b32 v45, v65\n\t"
"v_mov_b32 v46, v66\n\t"
"v_mfma_f32_16x16x32_bf16 v[72:75], a[220:223], v[204:207], v[72:75]\n\t"
"s_waitcnt lgkmcnt(0)\n\t"
"s_barrier\n\t"
"ds_read_b128 v[116:119], v19 offset:18432\n\t"
"v_mfma_f32_16x16x32_bf16 v[72:75], a[236:239], v[212:215], v[72:75]\n\t"
"ds_read_b128 v[144:147], v20 offset:20480\n\t"
"v_mov_b32 v47, v67\n\t"
"v_mfma_f32_16x16x32_bf16 v[72:75], a[252:255], v[176:179], v[72:75]\n\t"
"ds_read_b128 v[252:255], v21 offset:22528\n\t"
"v_mul_f32 v48, v64, v64\n\t"
"v_mfma_f32_16x16x32_bf16 v[76:79], a[252:255], v[196:199], v[76:79]\n\t"
"ds_read_b128 v[124:127], v19 offset:19456\n\t"
"v_mul_f32 v49, v65, v65\n\t"
"v_mfma_f32_16x16x32_bf16 v[76:79], a[236:239], v[184:187], v[76:79]\n\t"
"ds_read_b128 v[152:155], v20 offset:21504\n\t"
"v_mul_f32 v50, v66, v66\n\t"
"v_mfma_f32_16x16x32_bf16 v[76:79], a[220:223], v[176:179], v[76:79]\n\t"
"ds_read_b128 v[220:223], v21 offset:23552\n\t"
"v_mul_f32 v51, v67, v67\n\t"
"v_mfma_f32_16x16x32_bf16 v[76:79], a[204:207], v[212:215], v[76:79]\n\t"
"v_add_f32 v44, v44, v68\n\t"
"v_add_f32 v45, v45, v69\n\t"
"v_mfma_f32_16x16x32_bf16 v[76:79], a[188:191], v[204:207], v[76:79]\n\t"
"v_add_f32 v46, v46, v70\n\t"
"v_add_f32 v47, v47, v71\n\t"
"v_mfma_f32_16x16x32_bf16 v[76:79], a[172:175], v[192:195], v[76:79]\n\t"
"s_waitcnt lgkmcnt(4)\n\t"
"v_add_f32 v116, v116, v144\n\t"
"v_add_f32 v117, v117, v145\n\t"
"v_mfma_f32_16x16x32_bf16 v[80:83], a[172:175], v[212:215], v[80:83]\n\t"
"v_add_f32 v118, v118, v146\n\t"
"v_add_f32 v119, v119, v147\n\t"
"v_mfma_f32_16x16x32_bf16 v[80:83], a[188:191], v[176:179], v[80:83]\n\t"
"s_waitcnt lgkmcnt(3)\n\t"
"v_add_f32 v116, v116, v252\n\t"
"v_add_f32 v117, v117, v253\n\t"
"v_mfma_f32_16x16x32_bf16 v[80:83], a[204:207], v[184:187], v[80:83]\n\t"
"v_add_f32 v118, v118, v254\n\t"
"v_add_f32 v119, v119, v255\n\t"
"v_mfma_f32_16x16x32_bf16 v[80:83], a[220:223], v[196:199], v[80:83]\n\t"
"v_add_f32 v88, v88, v116\n\t"
"v_add_f32 v89, v89, v117\n\t"
"v_mfma_f32_16x16x32_bf16 v[80:83], a[236:239], v[200:203], v[80:83]\n\t"
"v_add_f32 v90, v90, v118\n\t"
"v_add_f32 v91, v91, v119\n\t"
"v_mfma_f32_16x16x32_bf16 v[80:83], a[252:255], v[208:211], v[80:83]\n\t"
"s_waitcnt lgkmcnt(1)\n\t"
"v_add_f32 v124, v124, v152\n\t"
"v_add_f32 v125, v125, v153\n\t"
"v_mfma_f32_16x16x32_bf16 v[84:87], a[252:255], v[224:227], v[84:87]\n\t"
"v_add_f32 v126, v126, v154\n\t"
"v_add_f32 v127, v127, v155\n\t"
"v_mfma_f32_16x16x32_bf16 v[84:87], a[236:239], v[216:219], v[84:87]\n\t"
"s_waitcnt lgkmcnt(0)\n\t"
"v_add_f32 v124, v124, v220\n\t"
"v_add_f32 v125, v125, v221\n\t"
"v_mfma_f32_16x16x32_bf16 v[84:87], a[220:223], v[208:211], v[84:87]\n\t"
"v_add_f32 v126, v126, v222\n\t"
"v_add_f32 v127, v127, v223\n\t"
"v_mfma_f32_16x16x32_bf16 v[84:87], a[204:207], v[200:203], v[84:87]\n\t"
"v_add_f32 v92, v92, v124\n\t"
"v_add_f32 v93, v93, v125\n\t"
"v_mfma_f32_16x16x32_bf16 v[84:87], a[188:191], v[196:199], v[84:87]\n\t"
"v_add_f32 v94, v94, v126\n\t"
"v_add_f32 v95, v95, v127\n\t"
"v_mfma_f32_16x16x32_bf16 v[84:87], a[172:175], v[184:187], v[84:87]\n\t"
"v_fmac_f32 v48, v68, v68\n\t"
"v_fmac_f32 v49, v69, v69\n\t"
"v_mfma_f32_16x16x32_bf16 v[88:91], a[172:175], v[200:203], v[88:91]\n\t"
"v_fmac_f32 v50, v70, v70\n\t"
"v_fmac_f32 v51, v71, v71\n\t"
"v_mfma_f32_16x16x32_bf16 v[88:91], a[188:191], v[208:211], v[88:91]\n\t"
"v_add_f32 v44, v44, v72\n\t"
"v_add_f32 v45, v45, v73\n\t"
"v_mfma_f32_16x16x32_bf16 v[88:91], a[204:207], v[216:219], v[88:91]\n\t"
"v_add_f32 v46, v46, v74\n\t"
"v_add_f32 v47, v47, v75\n\t"
"v_mfma_f32_16x16x32_bf16 v[88:91], a[220:223], v[224:227], v[88:91]\n\t"
"v_fmac_f32 v48, v72, v72\n\t"
"v_fmac_f32 v49, v73, v73\n\t"
"v_mfma_f32_16x16x32_bf16 v[88:91], a[236:239], v[228:231], v[88:91]\n\t"
"v_fmac_f32 v50, v74, v74\n\t"
"v_fmac_f32 v51, v75, v75\n\t"
"v_mfma_f32_16x16x32_bf16 v[88:91], a[252:255], v[236:239], v[88:91]\n\t"
"v_add_f32 v44, v44, v76\n\t"
"v_add_f32 v45, v45, v77\n\t"
"v_mfma_f32_16x16x32_bf16 v[92:95], a[252:255], v[120:123], v[92:95]\n\t"
"v_add_f32 v46, v46, v78\n\t"
"v_add_f32 v47, v47, v79\n\t"
"v_mfma_f32_16x16x32_bf16 v[92:95], a[236:239], v[248:251], v[92:95]\n\t"
"v_fmac_f32 v48, v76, v76\n\t"
"v_fmac_f32 v49, v77, v77\n\t"
"v_mfma_f32_16x16x32_bf16 v[92:95], a[220:223], v[236:239], v[92:95]\n\t"
"v_fmac_f32 v50, v78, v78\n\t"
"v_fmac_f32 v51, v79, v79\n\t"
"v_mfma_f32_16x16x32_bf16 v[92:95], a[204:207], v[228:231], v[92:95]\n\t"
"v_add_f32 v44, v44, v80\n\t"
"v_add_f32 v45, v45, v81\n\t"
"v_mfma_f32_16x16x32_bf16 v[92:95], a[188:191], v[224:227], v[92:95]\n\t"
"v_add_f32 v46, v46, v82\n\t"
"v_add_f32 v47, v47, v83\n\t"
"v_mfma_f32_16x16x32_bf16 v[92:95], a[172:175], v[216:219], v[92:95]\n\t"
"v_pk_fma_f32 v[48:49], v[80:81], v[80:81], v[48:49]\n\t"
"v_pk_fma_f32 v[50:51], v[82:83], v[82:83], v[50:51]\n\t"
"v_pk_add_f32 v[44:45], v[44:45], v[84:85]\n\t"
"v_pk_add_f32 v[46:47], v[46:47], v[86:87]\n\t"
"v_pk_fma_f32 v[48:49], v[84:85], v[84:85], v[48:49]\n\t"
"v_pk_fma_f32 v[50:51], v[86:87], v[86:87], v[50:51]\n\t"
"v_pk_add_f32 v[44:45], v[44:45], v[88:89]\n\t"
"v_pk_add_f32 v[46:47], v[46:47], v[90:91]\n\t"
"v_pk_fma_f32 v[48:49], v[88:89], v[88:89], v[48:49]\n\t"
"v_pk_fma_f32 v[50:51], v[90:91], v[90:91], v[50:51]\n\t"
"v_pk_add_f32 v[44:45], v[44:45], v[92:93]\n\t"
"v_pk_add_f32 v[46:47], v[46:47], v[94:95]\n\t"
"v_pk_fma_f32 v[48:49], v[92:93], v[92:93], v[48:49]\n\t"
"v_pk_fma_f32 v[50:51], v[94:95], v[94:95], v[50:51]\n\t"
"s_mov_b64 exec, s[30:31]\n\t"
"v_mov_b64 v[44:45], v[32:33]\n\t"
"v_mov_b64 v[46:47], v[34:35]\n\t"
"v_mov_b64 v[48:49], v[58:59]\n\t"
"v_mov_b64 v[50:51], v[60:61]\n\t"
"s_mov_b64 exec, -1\n\t"
"v_add_f32_dpp v54, v44, v44 row_ror:8 row_mask:0xf bank_mask:0x3\n\t"
"v_add_f32_dpp v54, v46, v46 row_ror:8 row_mask:0xf bank_mask:0xc\n\t"
"v_add_f32_dpp v55, v45, v45 row_ror:8 row_mask:0xf bank_mask:0x3\n\t"
"v_add_f32_dpp v55, v47, v47 row_ror:8 row_mask:0xf bank_mask:0xc\n\t"
"v_add_f32_dpp v56, v48, v48 row_ror:8 row_mask:0xf bank_mask:0x3\n\t"
"v_add_f32_dpp v56, v50, v50 row_ror:8 row_mask:0xf bank_mask:0xc\n\t"
"v_add_f32_dpp v57, v49, v49 row_ror:8 row_mask:0xf bank_mask:0x3\n\t"
"v_add_f32_dpp v57, v51, v51 row_ror:8 row_mask:0xf bank_mask:0xc\n\t"
"v_add_f32_dpp v52, v54, v54 row_shl:4 row_mask:0xf bank_mask:0x5\n\t"
"v_add_f32_dpp v52, v55, v55 row_shr:4 row_mask:0xf bank_mask:0xa\n\t"
"v_add_f32_dpp v53, v56, v56 row_shl:4 row_mask:0xf bank_mask:0x5\n\t"
"v_add_f32_dpp v53, v57, v57 row_shr:4 row_mask:0xf bank_mask:0xa\n\t"
"v_nop_e64\n\t"
"v_add_f32_dpp v52, v52, v52 quad_perm:[1,0,3,2] row_mask:0xf bank_mask:0xf\n\t"
"v_add_f32_dpp v53, v53, v53 quad_perm:[1,0,3,2] row_mask:0xf bank_mask:0xf\n\t"
"v_nop_e64\n\t"
"v_add_f32_dpp v52, v52, v52 quad_perm:[2,3,0,1] row_mask:0xf bank_mask:0xf\n\t"
"v_add_f32_dpp v53, v53, v53 quad_perm:[2,3,0,1] row_mask:0xf bank_mask:0xf\n\t"
"v_mul_f32 v54, s39, v52\n\t"
"s_mov_b64 exec, s[36:37]\n\t"
"v_add_f32_e64 v96, v39, v54\n\t"
"v_fma_f32 v98, -v52, v54, v53\n\t"
"buffer_store_dwordx4 v[96:99], v29, s[16:19], 0 offen sc1\n\t"
"s_mov_b64 exec, -1\n\t"
"s_mov_b32 s43, 100000\n\t"
"buffer_load_dword v24, v29, s[16:19], 0 offen sc1\n\t"
"s_and_b32 s40, s34, 1\n\t"
"s_lshl_b32 s40, s40, 7\n\t"
"v_lshrrev_b32 v103, 5, v1\n\t"
"v_lshl_or_b32 v103, v103, 6, s40\n\t"
"v_xor_b32_e64 v100, v103, v18\n\t"
"ds_write_b128 v100, v[64:67] offset:24576\n\t"
"ds_write_b128 v100, v[68:71] offset:25600\n\t"
"ds_write_b128 v100, v[72:75] offset:26624\n\t"
"ds_write_b128 v100, v[76:79] offset:27648\n\t"
"ds_write_b128 v100, v[80:83] offset:28672\n\t"
"ds_write_b128 v100, v[84:87] offset:29696\n\t"
"ds_write_b128 v100, v[88:91] offset:30720\n\t"
"ds_write_b128 v100, v[92:95] offset:31744\n\t"
"v_bfe_u32 v103, v1, 1, 2\n\t"
"v_mul_u32_u24 v3, 40960, v103\n\t"
"v_lshrrev_b32 v2, 3, v1\n\t"
"v_lshlrev_b32 v101, 4, v2\n\t"
"v_and_b32 v2, 3, v1\n\t"
"v_lshlrev_b32 v2, 6, v2\n\t"
"v_xor_b32 v101, v2, v101\n\t"
"v_and_b32 v2, 1, v1\n\t"
"v_lshl_or_b32 v101, v2, 9, v101\n\t"
"s_lshl_b32 s40, s34, 11\n\t"
"v_add3_u32 v101, v101, v3, s40\n\t"
"v_xor_b32 v102, 128, v101\n\t"
"v_and_b32 v103, 7, v1\n\t"
"v_lshl_add_u32 v3, v103, 5, v3\n\t"
"s_mul_i32 s40, s34, 41024\n\t"
"s_add_u32 s40, s40, 32768\n\t"
"v_and_b32_e64 v103, 15, v1\n\t"
"v_lshl_add_u32 v2, v103, 2, s40\n\t"
"v_lshlrev_b32_e64 v103, 4, v103\n\t"
"ds_bpermute_b32 v40, v103, v39\n\t"
"v_mov_b32 v41, 0x3627c5ac\n\t"
"s_mov_b32 s41, 0x3d838849\n\t"
"s_mov_b32 s42, 0x3a038849\n\t"
"s_waitcnt lgkmcnt(0)\n\t"
"s_barrier\n\t"
"ds_read_b128 v[64:67], v101 offset:24576\n\t"
"ds_read_b128 v[68:71], v101 offset:24832\n\t"
"ds_read_b128 v[72:75], v101 offset:25600\n\t"
"ds_read_b128 v[76:79], v101 offset:25856\n\t"
"ds_read_b128 v[80:83], v102 offset:24576\n\t"
"ds_read_b128 v[84:87], v102 offset:24832\n\t"
"ds_read_b128 v[88:91], v102 offset:25600\n\t"
"ds_read_b128 v[92:95], v102 offset:25856\n\t"
"v_mov_b32 v32, 0xbdd2d3e8\n\t"
"v_mov_b32 v33, 0xbdd2d3e8\n\t"
"v_mov_b32 v34, 0xc0135761\n\t"
"v_mov_b32 v35, 0xc0135761\n\t"
"s_mov_b32 s40, 0x10000\n\t"
"s_waitcnt lgkmcnt(0)\n\t"
"buffer_load_dwordx4 v[116:119], v30, s[16:19], 0 offen offset:256 sc1\n\t"
"buffer_load_dwordx4 v[120:123], v30, s[16:19], 0 offen offset:1280 sc1\n\t"
"buffer_load_dwordx4 v[124:127], v30, s[16:19], 0 offen offset:2304 sc1\n\t"
"buffer_load_dwordx4 v[128:131], v30, s[16:19], 0 offen offset:3328 sc1\n\t"
"s_branch PL_%=\n\t"
"RT_%=:\n\t"
"s_mov_b64 exec, s[4:5]\n\t"
"buffer_load_dwordx4 v[116:119], v30, s[16:19], 0 offen offset:256 sc1\n\t"
"buffer_load_dwordx4 v[120:123], v30, s[16:19], 0 offen offset:1280 sc1\n\t"
"buffer_load_dwordx4 v[124:127], v30, s[16:19], 0 offen offset:2304 sc1\n\t"
"buffer_load_dwordx4 v[128:131], v30, s[16:19], 0 offen offset:3328 sc1\n\t"
"s_sub_u32 s43, s43, 1\n\t"
"s_cmp_lg_u32 s43, 0\n\t"
"s_cbranch_scc0 PD_%=\n\t"
"PL_%=:\n\t"
"s_waitcnt vmcnt(3)\n\t"
"v_cmpx_eq_u32 vcc, v119, v26\n\t"
"v_sub_f32 v54, v116, v40\n\t"
"v_cmpx_eq_u32 vcc, v117, v26\n\t"
"v_mul_f32_e64 v44, s41, v54\n\t"
"v_fma_f32 v45, s42, v118, v41\n\t"
"v_fmac_f32 v45, v44, v54\n\t"
"s_waitcnt vmcnt(2)\n\t"
"v_cmpx_eq_u32 vcc, v123, v26\n\t"
"v_sub_f32 v54, v120, v40\n\t"
"v_cmpx_eq_u32 vcc, v121, v26\n\t"
"v_mul_f32 v55, s41, v54\n\t"
"v_fma_f32 v45, s42, v122, v45\n\t"
"v_add_f32 v44, v44, v55\n\t"
"v_fmac_f32 v45, v55, v54\n\t"
"s_waitcnt vmcnt(1)\n\t"
"v_cmpx_eq_u32 vcc, v127, v26\n\t"
"v_sub_f32 v54, v124, v40\n\t"
"v_cmpx_eq_u32 vcc, v125, v26\n\t"
"v_mul_f32_e64 v55, s41, v54\n\t"
"v_fma_f32 v45, s42, v126, v45\n\t"
"v_add_f32 v44, v44, v55\n\t"
"v_fmac_f32 v45, v55, v54\n\t"
"s_waitcnt vmcnt(0)\n\t"
"v_cmpx_eq_u32 vcc, v131, v26\n\t"
"v_sub_f32 v54, v128, v40\n\t"
"v_cmpx_eq_u32 vcc, v129, v26\n\t"
"v_mul_f32_e64 v55, v38, v54\n\t"
"v_fma_f32 v45, s42, v130, v45\n\t"
"s_andn2_b64 s[4:5], s[4:5], exec\n\t"
"v_add_f32 v44, v44, v55\n\t"
"v_fmac_f32 v45, v55, v54\n\t"
"s_cbranch_scc1 RT_%=\n\t"
"PD_%=:\n\t"
"s_mov_b64 exec, -1\n\t"
"v_permlane32_swap_b32 v44, v45\n\t"
"v_add_f32 v46, v44, v45\n\t"
"v_add_f32 v47, v44, v45\n\t"
"s_nop 1\n\t"
"v_permlane16_swap_b32 v46, v47\n\t"
"v_add_f32 v52, v46, v47\n\t"
"v_add_f32 v55, v46, v47\n\t"
"s_nop 1\n\t"
"v_permlane32_swap_b32 v52, v55\n\t"
"v_fma_f32 v55, -v52, v52, v55\n\t"
"v_rsq_f32 v55, v55\n\t"
"s_or_b32 s43, s4, s5\n\t"
"s_or_b32 s43, s43, s38\n\t"
"v_mul_f32 v55, v55, v27\n\t"
"s_cmp_eq_u32 s43, 0\n\t"
"v_fma_f32 v54, -v52, v55, v28\n\t"
"ds_write2_b32 v2, v55, v54 offset0:0 offset1:16\n\t"
"s_cselect_b64 exec, 1, 0\n\t"
"buffer_store_dword v26, off, s[16:19], 0 sc1\n\t"
"s_mov_b64 exec, -1\n\t"
"s_waitcnt lgkmcnt(0)\n\t"
"s_barrier\n\t"
"ds_read_b128 v[100:103], v3 offset:32768\n\t"
"ds_read_b128 v[108:111], v3 offset:32832\n\t"
"ds_read_b128 v[104:107], v3 offset:32784\n\t"
"ds_read_b128 v[112:115], v3 offset:32848\n\t"
"s_waitcnt lgkmcnt(2)\n\t"
"v_pk_fma_f32 v[64:65], v[64:65], v[100:101], v[108:109]\n\t"
"v_pk_fma_f32 v[66:67], v[66:67], v[102:103], v[110:111]\n\t"
"v_pk_mul_f32 v[180:181], v[64:65], v[64:65]\n\t"
"v_pk_mul_f32 v[182:183], v[66:67], v[66:67]\n\t"
"v_pk_fma_f32 v[180:181], v[180:181], v[32:33], v[34:35]\n\t"
"v_pk_fma_f32 v[182:183], v[182:183], v[32:33], v[34:35]\n\t"
"v_pk_mul_f32 v[180:181], v[180:181], v[64:65]\n\t"
"v_pk_mul_f32 v[182:183], v[182:183], v[66:67]\n\t"
"v_exp_f32 v180, v180\n\t"
"v_exp_f32 v181, v181\n\t"
"s_waitcnt lgkmcnt(0)\n\t"
"v_exp_f32 v182, v182\n\t"
"v_exp_f32_e64 v183, v183\n\t"
"v_pk_add_f32 v[180:181], v[180:181], 1.0 op_sel_hi:[1,0]\n\t"
"v_pk_add_f32 v[182:183], v[182:183], 1.0 op_sel_hi:[1,0]\n\t"
"v_rcp_f32 v180, v180\n\t"
"v_rcp_f32 v181, v181\n\t"
"v_pk_fma_f32 v[68:69], v[68:69], v[104:105], v[112:113]\n\t"
"v_rcp_f32 v182, v182\n\t"
"v_rcp_f32 v183, v183\n\t"
"v_pk_mul_f32 v[180:181], v[180:181], v[64:65]\n\t"
"v_pk_mul_f32 v[182:183], v[182:183], v[66:67]\n\t"
"v_cvt_pk_bf16_f32 v212, v180, v181\n\t"
"v_cvt_pk_bf16_f32 v213, v182, v183\n\t"
"v_pk_mul_f32 v[184:185], v[68:69], v[68:69]\n\t"
"v_pk_fma_f32 v[70:71], v[70:71], v[106:107], v[114:115]\n\t"
"v_pk_fma_f32 v[184:185], v[184:185], v[32:33], v[34:35]\n\t"
"v_pk_mul_f32 v[186:187], v[70:71], v[70:71]\n\t"
"v_pk_mul_f32 v[184:185], v[184:185], v[68:69]\n\t"
"v_pk_fma_f32 v[186:187], v[186:187], v[32:33], v[34:35]\n\t"
"v_exp_f32 v184, v184\n\t"
"v_exp_f32 v185, v185\n\t"
"v_pk_mul_f32 v[186:187], v[186:187], v[70:71]\n\t"
"v_pk_add_f32 v[184:185], v[184:185], 1.0 op_sel_hi:[1,0]\n\t"
"v_exp_f32 v186, v186\n\t"
"v_exp_f32 v187, v187\n\t"
"v_pk_fma_f32 v[72:73], v[72:73], v[100:101], v[108:109]\n\t"
"v_rcp_f32 v184, v184\n\t"
"v_rcp_f32 v185, v185\n\t"
"v_pk_add_f32 v[186:187], v[186:187], 1.0 op_sel_hi:[1,0]\n\t"
"v_pk_mul_f32 v[184:185], v[184:185], v[68:69]\n\t"
"v_rcp_f32 v186, v186\n\t"
"v_rcp_f32 v187, v187\n\t"
"v_cvt_pk_bf16_f32 v214, v184, v185\n\t"
"v_pk_mul_f32 v[186:187], v[186:187], v[70:71]\n\t"
"v_pk_mul_f32 v[188:189], v[72:73], v[72:73]\n\t"
"v_cvt_pk_bf16_f32 v215, v186, v187\n\t"
"buffer_store_dwordx4 v[212:215], v31, s[20:23], 0 offen offset:0 sc1\n\t"
"v_pk_fma_f32 v[188:189], v[188:189], v[32:33], v[34:35]\n\t"
"v_pk_fma_f32 v[74:75], v[74:75], v[102:103], v[110:111]\n\t"
"v_pk_mul_f32 v[188:189], v[188:189], v[72:73]\n\t"
"v_pk_mul_f32 v[190:191], v[74:75], v[74:75]\n\t"
"v_exp_f32 v188, v188\n\t"
"v_exp_f32 v189, v189\n\t"
"v_pk_fma_f32 v[190:191], v[190:191], v[32:33], v[34:35]\n\t"
"v_pk_add_f32 v[188:189], v[188:189], 1.0 op_sel_hi:[1,0]\n\t"
"v_pk_mul_f32 v[190:191], v[190:191], v[74:75]\n\t"
"v_rcp_f32 v188, v188\n\t"
"v_rcp_f32 v189, v189\n\t"
"v_pk_fma_f32 v[76:77], v[76:77], v[104:105], v[112:113]\n\t"
"v_exp_f32 v190, v190\n\t"
"v_exp_f32 v191, v191\n\t"
"v_pk_mul_f32 v[188:189], v[188:189], v[72:73]\n\t"
"v_pk_add_f32 v[190:191], v[190:191], 1.0 op_sel_hi:[1,0]\n\t"
"v_cvt_pk_bf16_f32 v216, v188, v189\n\t"
"v_rcp_f32 v190, v190\n\t"
"v_rcp_f32 v191, v191\n\t"
"v_pk_mul_f32 v[192:193], v[76:77], v[76:77]\n\t"
"v_pk_mul_f32 v[190:191], v[190:191], v[74:75]\n\t"
"v_pk_fma_f32 v[192:193], v[192:193], v[32:33], v[34:35]\n\t"
"v_cvt_pk_bf16_f32 v217, v190, v191\n\t"
"v_pk_mul_f32 v[192:193], v[192:193], v[76:77]\n\t"
"v_pk_fma_f32 v[78:79], v[78:79], v[106:107], v[114:115]\n\t"
"v_exp_f32 v192, v192\n\t"
"v_exp_f32 v193, v193\n\t"
"v_pk_mul_f32 v[194:195], v[78:79], v[78:79]\n\t"
"v_pk_add_f32 v[192:193], v[192:193], 1.0 op_sel_hi:[1,0]\n\t"
"v_pk_fma_f32 v[194:195], v[194:195], v[32:33], v[34:35]\n\t"
"v_rcp_f32 v192, v192\n\t"
"v_rcp_f32 v193, v193\n\t"
"v_pk_mul_f32 v[194:195], v[194:195], v[78:79]\n\t"
"v_pk_mul_f32 v[192:193], v[192:193], v[76:77]\n\t"
"v_exp_f32 v194, v194\n\t"
"v_exp_f32 v195, v195\n\t"
"v_cvt_pk_bf16_f32 v218, v192, v193\n\t"
"v_pk_add_f32 v[194:195], v[194:195], 1.0 op_sel_hi:[1,0]\n\t"
"v_pk_fma_f32 v[80:81], v[80:81], v[100:101], v[108:109]\n\t"
"v_rcp_f32 v194, v194\n\t"
"v_rcp_f32 v195, v195\n\t"
"v_pk_mul_f32 v[196:197], v[80:81], v[80:81]\n\t"
"v_pk_mul_f32 v[194:195], v[194:195], v[78:79]\n\t"
"v_pk_fma_f32 v[196:197], v[196:197], v[32:33], v[34:35]\n\t"
"v_cvt_pk_bf16_f32 v219, v194, v195\n\t"
"buffer_store_dwordx4 v[216:219], v31, s[20:23], 0 offen offset:1024 sc1\n\t"
"v_pk_mul_f32 v[196:197], v[196:197], v[80:81]\n\t"
"v_pk_fma_f32 v[82:83], v[82:83], v[102:103], v[110:111]\n\t"
"v_exp_f32 v196, v196\n\t"
"v_exp_f32 v197, v197\n\t"
"v_pk_mul_f32 v[198:199], v[82:83], v[82:83]\n\t"
"v_pk_add_f32 v[196:197], v[196:197], 1.0 op_sel_hi:[1,0]\n\t"
"v_pk_fma_f32 v[198:199], v[198:199], v[32:33], v[34:35]\n\t"
"v_rcp_f32 v196, v196\n\t"
"v_rcp_f32 v197, v197\n\t"
"v_pk_mul_f32 v[198:199], v[198:199], v[82:83]\n\t"
"v_pk_mul_f32 v[196:197], v[196:197], v[80:81]\n\t"
"v_exp_f32 v198, v198\n\t"
"v_exp_f32 v199, v199\n\t"
"v_cvt_pk_bf16_f32 v220, v196, v197\n\t"
"v_pk_add_f32 v[198:199], v[198:199], 1.0 op_sel_hi:[1,0]\n\t"
"v_pk_fma_f32 v[84:85], v[84:85], v[104:105], v[112:113]\n\t"
"v_rcp_f32 v198, v198\n\t"
"v_rcp_f32 v199, v199\n\t"
"v_pk_mul_f32 v[200:201], v[84:85], v[84:85]\n\t"
"v_pk_mul_f32 v[198:199], v[198:199], v[82:83]\n\t"
"v_pk_fma_f32 v[200:201], v[200:201], v[32:33], v[34:35]\n\t"
"v_cvt_pk_bf16_f32 v221, v198, v199\n\t"
"v_pk_mul_f32 v[200:201], v[200:201], v[84:85]\n\t"
"v_pk_fma_f32 v[86:87], v[86:87], v[106:107], v[114:115]\n\t"
"v_exp_f32 v200, v200\n\t"
"v_exp_f32 v201, v201\n\t"
"v_pk_mul_f32 v[202:203], v[86:87], v[86:87]\n\t"
"v_pk_add_f32 v[200:201], v[200:201], 1.0 op_sel_hi:[1,0]\n\t"
"v_pk_fma_f32 v[202:203], v[202:203], v[32:33], v[34:35]\n\t"
"v_rcp_f32 v200, v200\n\t"
"v_rcp_f32 v201, v201\n\t"
"v_pk_mul_f32 v[202:203], v[202:203], v[86:87]\n\t"
"v_pk_mul_f32 v[200:201], v[200:201], v[84:85]\n\t"
"v_exp_f32 v202, v202\n\t"
"v_exp_f32 v203, v203\n\t"
"v_cvt_pk_bf16_f32 v222, v200, v201\n\t"
"v_pk_add_f32 v[202:203], v[202:203], 1.0 op_sel_hi:[1,0]\n\t"
"v_pk_fma_f32 v[88:89], v[88:89], v[100:101], v[108:109]\n\t"
"v_rcp_f32 v202, v202\n\t"
"v_rcp_f32 v203, v203\n\t"
"v_pk_mul_f32 v[204:205], v[88:89], v[88:89]\n\t"
"v_pk_mul_f32 v[202:203], v[202:203], v[86:87]\n\t"
"v_pk_fma_f32 v[204:205], v[204:205], v[32:33], v[34:35]\n\t"
"v_cvt_pk_bf16_f32 v223, v202, v203\n\t"
"buffer_store_dwordx4 v[220:223], v31, s[20:23], s40 offen offset:0 sc1\n\t"
"v_pk_mul_f32 v[204:205], v[204:205], v[88:89]\n\t"
"v_pk_fma_f32 v[90:91], v[90:91], v[102:103], v[110:111]\n\t"
"v_exp_f32 v204, v204\n\t"
"v_exp_f32 v205, v205\n\t"
"v_pk_mul_f32 v[206:207], v[90:91], v[90:91]\n\t"
"v_pk_add_f32 v[204:205], v[204:205], 1.0 op_sel_hi:[1,0]\n\t"
"v_pk_fma_f32 v[206:207], v[206:207], v[32:33], v[34:35]\n\t"
"v_rcp_f32 v204, v204\n\t"
"v_rcp_f32 v205, v205\n\t"
"v_pk_mul_f32 v[206:207], v[206:207], v[90:91]\n\t"
"v_pk_mul_f32 v[204:205], v[204:205], v[88:89]\n\t"
"v_exp_f32 v206, v206\n\t"
"v_exp_f32 v207, v207\n\t"
"v_cvt_pk_bf16_f32 v224, v204, v205\n\t"
"v_pk_add_f32 v[206:207], v[206:207], 1.0 op_sel_hi:[1,0]\n\t"
"v_pk_fma_f32 v[92:93], v[92:93], v[104:105], v[112:113]\n\t"
"v_rcp_f32 v206, v206\n\t"
"v_rcp_f32 v207, v207\n\t"
"v_pk_mul_f32 v[208:209], v[92:93], v[92:93]\n\t"
"v_pk_mul_f32 v[206:207], v[206:207], v[90:91]\n\t"
"v_pk_fma_f32 v[208:209], v[208:209], v[32:33], v[34:35]\n\t"
"v_cvt_pk_bf16_f32 v225, v206, v207\n\t"
"v_pk_mul_f32 v[208:209], v[208:209], v[92:93]\n\t"
"v_pk_fma_f32 v[94:95], v[94:95], v[106:107], v[114:115]\n\t"
"v_exp_f32 v208, v208\n\t"
"v_exp_f32 v209, v209\n\t"
"v_pk_mul_f32 v[210:211], v[94:95], v[94:95]\n\t"
"v_pk_add_f32 v[208:209], v[208:209], 1.0 op_sel_hi:[1,0]\n\t"
"v_pk_fma_f32 v[210:211], v[210:211], v[32:33], v[34:35]\n\t"
"v_rcp_f32 v208, v208\n\t"
"v_rcp_f32 v209, v209\n\t"
"v_pk_mul_f32 v[210:211], v[210:211], v[94:95]\n\t"
"v_pk_mul_f32 v[208:209], v[208:209], v[92:93]\n\t"
"v_exp_f32 v210, v210\n\t"
"v_exp_f32 v211, v211\n\t"
"v_cvt_pk_bf16_f32 v226, v208, v209\n\t"
"v_pk_add_f32 v[210:211], v[210:211], 1.0 op_sel_hi:[1,0]\n\t"
"s_nop 0\n\t"
"v_rcp_f32 v210, v210\n\t"
"v_rcp_f32 v211, v211\n\t"
"s_nop 0\n\t"
"v_pk_mul_f32 v[210:211], v[210:211], v[94:95]\n\t"
"v_nop_e64\n\t"
"v_cvt_pk_bf16_f32 v227, v210, v211\n\t"
"buffer_store_dwordx4 v[224:227], v31, s[20:23], s40 offen offset:1024 sc1\n\t"
      :
      : "s"(__builtin_amdgcn_kernarg_segment_ptr()), "s"(blockIdx.x), "v"(threadIdx.x)
      : "memory", "a0","a1","a2","a3","a4","a5","a6","a7","a8","a9","a10","a11","a12","a13","a14","a15","a16","a17","a18","a19","a20","a21","a22","a23","a24","a25","a26","a27","a28","a29","a30","a31","a32","a33","a34","a35","a36","a37","a38","a39","a40","a41","a42","a43","a44","a45","a46","a47","a48","a49","a50","a51","a52","a53","a54","a55","a56","a57","a58","a59","a60","a61","a62","a63","a64","a65","a66","a67","a68","a69","a70","a71","a72","a73","a74","a75","a76","a77","a78","a79","a80","a81","a82","a83","a84","a85","a86","a87","a88","a89","a90","a91","a92","a93","a94","a95","a96","a97","a98","a99","a100","a101","a102","a103","a104","a105","a106","a107","a108","a109","a110","a111","a112","a113","a114","a115","a116","a117","a118","a119","a120","a121","a122","a123","a124","a125","a126","a127","a128","a129","a130","a131","a132","a133","a134","a135","a136","a137","a138","a139","a140","a141","a142","a143","a144","a145","a146","a147","a148","a149","a150","a151","a152","a153","a154","a155","a156","a157","a158","a159","a160","a161","a162","a163","a164","a165","a166","a167","a168","a169","a170","a171","a172","a173","a174","a175","a176","a177","a178","a179","a180","a181","a182","a183","a184","a185","a186","a187","a188","a189","a190","a191","a192","a193","a194","a195","a196","a197","a198","a199","a200","a201","a202","a203","a204","a205","a206","a207","a208","a209","a210","a211","a212","a213","a214","a215","a216","a217","a218","a219","a220","a221","a222","a223","a224","a225","a226","a227","a228","a229","a230","a231","a232","a233","a234","a235","a236","a237","a238","a239","a240","a241","a242","a243","a244","a245","a246","a247","a248","a249","a250","a251","a252","a253","a254","a255","v1","v2","v3","v4","v5","v6","v7","v8","v9","v10","v11","v12","v13","v14","v15","v16","v17","v18","v19","v20","v21","v22","v23","v24","v25","v26","v27","v28","v29","v30","v31","v32","v33","v34","v35","v36","v37","v38","v39","v40","v41","v42","v43","v44","v45","v46","v47","v48","v49","v50","v51","v52","v53","v54","v55","v56","v57","v58","v59","v60","v61","v62","v63","v64","v65","v66","v67","v68","v69","v70","v71","v72","v73","v74","v75","v76","v77","v78","v79","v80","v81","v82","v83","v84","v85","v86","v87","v88","v89","v90","v91","v92","v93","v94","v95","v96","v97","v98","v99","v100","v101","v102","v103","v104","v105","v106","v107","v108","v109","v110","v111","v112","v113","v114","v115","v116","v117","v118","v119","v120","v121","v122","v123","v124","v125","v126","v127","v128","v129","v130","v131","v132","v133","v134","v135","v136","v137","v138","v139","v140","v141","v142","v143","v144","v145","v146","v147","v148","v149","v150","v151","v152","v153","v154","v155","v156","v157","v158","v159","v160","v161","v162","v163","v164","v165","v166","v167","v168","v169","v170","v171","v172","v173","v174","v175","v176","v177","v178","v179","v180","v181","v182","v183","v184","v185","v186","v187","v188","v189","v190","v191","v192","v193","v194","v195","v196","v197","v198","v199","v200","v201","v202","v203","v204","v205","v206","v207","v208","v209","v210","v211","v212","v213","v214","v215","v216","v217","v218","v219","v220","v221","v222","v223","v224","v225","v226","v227","v228","v229","v230","v231","v232","v233","v234","v235","v236","v237","v238","v239","v240","v241","v242","v243","v244","v245","v246","v247","v248","v249","v250","v251","v252","v253","v254","v255","s3","s4","s5","s6","s7","s8","s9","s10","s11","s12","s13","s14","s15","s16","s17","s18","s19","s20","s21","s22","s23","s24","s25","s26","s27","s28","s29","s30","s31","s33","s34","s35","s36","s37","s38","s39","s40","s41","s42","s43","s88","s89","s90","s91","s92","s93","s94","s95","s96","s97","s98","s99","scc","vcc");
}

extern "C" void kernel_launch(void* const* d_in, const int* in_sizes, int n_in, void* d_out, int out_size, void* d_ws, size_t ws_size, hipStream_t stream) {
  (void)hipFuncSetAttribute((const void*)conv_gn_gelu_k, hipFuncAttributeMaxDynamicSharedMemorySize, LDS_BYTES);
  conv_gn_gelu_k<<<256, 256, LDS_BYTES, stream>>>((const unsigned short*)d_in[0], (const unsigned short*)d_in[1], (const unsigned short*)d_in[2],
                                                  (const unsigned short*)d_in[3], (const unsigned short*)d_in[4], (unsigned short*)d_out, (char*)d_ws);
}
```
